```python
import torch

def _softmax(x, axis=-1):
    return torch.softmax(x, dim=axis)
def _gelu(x):
    return torch.nn.functional.gelu(x, approximate="tanh")
def _silu(x):
    return torch.nn.functional.silu(x)
def _sigmoid(x):
    return torch.sigmoid(x)
def _layernorm(x, axis=-1, eps=1e-5):
    m = x.mean(dim=axis, keepdim=True)
    v = x.var(dim=axis, keepdim=True, unbiased=False)
    return (x - m) / torch.sqrt(v + eps)
def _scatter_add(tgt, idx, src, axis=0):
    return tgt.clone().index_add_(axis, idx, src)
def _segment_sum(src, seg, n):
    out = torch.zeros((n,) + src.shape[1:], dtype=src.dtype)
    return out.index_add_(0, seg, src)
def _pad(x, pw):
    flat = tuple(v for pair in reversed(pw) for v in pair)
    return torch.nn.functional.pad(x, flat)

kernel_name = 'standalone__kv_cache_quant_int4_int2_kv_with_per_channel_or_per_token_scales__decode'

def setup_inputs():

    _w = (lambda a: (lambda t: t.to(torch.bfloat16) if t.is_floating_point() else t)(torch.as_tensor(a)))
    B = 8
    Sq = 1
    Skv = 4096
    Hq = 16
    Hkv = 8
    Dh = 256
    bits = 3
    G = 128
    if G <= 0 or G >= Skv:
        G = max(1, Skv // 2)
    Skv = (Skv // G) * G
    ngk = Skv // G
    qmax = (1 << bits) - 1
    return {
        "q": _w((lambda *s: torch.randn(*s, dtype=torch.bfloat16) * 3.2)(B, Hq, Sq, Dh)),
        "qk": _w(torch.randint(0, qmax + 1, (B, Hkv, Skv, Dh)).to(torch.int32)),
        "qv": _w(torch.randint(0, qmax + 1, (B, Hkv, Skv, Dh)).to(torch.int32)),
        "k_scales": _w(torch.abs((lambda *s: torch.randn(*s, dtype=torch.bfloat16) * 3.2)(B, Hkv, ngk, Dh)) * 0.05 + 0.02),
        "k_zeros": _w((lambda *s: torch.randn(*s, dtype=torch.bfloat16) * 3.2)(B, Hkv, ngk, Dh) + float(qmax) / 2.0),
        "v_scales": _w(torch.abs((lambda *s: torch.randn(*s, dtype=torch.bfloat16) * 0.05)(B, Hkv, Skv, 1)) * 0.05 + 0.02),
        "v_zeros": _w((lambda *s: torch.randn(*s, dtype=torch.bfloat16) * 0.05)(B, Hkv, Skv, 1) + float(qmax) / 2.0),
    }

def reference(q, qk, qv, k_scales, k_zeros, v_scales, v_zeros):
    B, Hq, Sq, Dh = q.shape
    Hkv, Skv = qk.shape[1], qk.shape[2]
    ngk = k_scales.shape[2]
    G = Skv // ngk
    ks = (lambda x, n, axis: torch.repeat_interleave(x, n, dim=axis))(k_scales, G, axis=2)
    kz = (lambda x, n, axis: torch.repeat_interleave(x, n, dim=axis))(k_zeros, G, axis=2)
    k = ((lambda x, dt: x.to(dt))(qk, q.dtype) - kz) * ks
    v = ((lambda x, dt: x.to(dt))(qv, q.dtype) - v_zeros) * v_scales
    rep = Hq // Hkv
    if rep > 1:
        k = (lambda x, n, axis: torch.repeat_interleave(x, n, dim=axis))(k, rep, axis=1)
        v = (lambda x, n, axis: torch.repeat_interleave(x, n, dim=axis))(v, rep, axis=1)
    scale = float(Dh) ** -0.5
    logits = torch.einsum("bhqd,bhkd->bhqk", q, k) * scale
    p = _softmax(logits, axis=-1)
    o = torch.einsum("bhqk,bhkd->bhqd", p, v)
    return o
```

```cpp
#include <hip/hip_runtime.h>
#include <hip/hip_bf16.h>
#include <stdint.h>

#define CFG_WAVES 4
#define CFG_NC 2
#define CFG_W 16
#define CFG_NT 1
#define CFG_MIMIC 0
#define CFG_MINW 3

namespace {

constexpr int B = 8, HQ = 16, HKV = 8, SKV = 4096, DH = 256, G = 128, NG = SKV / G, REP = HQ / HKV;
constexpr int WAVES = CFG_WAVES, NC = CFG_NC, W = CFG_W, CH = 32;
constexpr int TPW = NC * CH;
constexpr int TPB = TPW * WAVES;
constexpr int NSPLIT = SKV / TPB;
constexpr int NPAIR = B * HKV;
static_assert(TPW <= G && G % TPW == 0, "a wave stays inside one K group");
static_assert((2 * CH) % W == 0, "ring divides the chunk body");
static_assert(REP == 2, "two query heads per kv head");

typedef int i32x4 __attribute__((ext_vector_type(4)));
typedef unsigned u32x2 __attribute__((ext_vector_type(2)));
typedef float f32x2 __attribute__((ext_vector_type(2)));
typedef float f32x4 __attribute__((ext_vector_type(4)));
typedef __bf16 bf16x2 __attribute__((ext_vector_type(2)));

__device__ __forceinline__ float bf_lo(unsigned u) { return __builtin_bit_cast(float, u << 16); }
__device__ __forceinline__ float bf_hi(unsigned u) { return __builtin_bit_cast(float, u & 0xffff0000u); }
__device__ __forceinline__ unsigned cvt_pk(float lo, float hi) {
  f32x2 v = {lo, hi};
  bf16x2 r = __builtin_convertvector(v, bf16x2);
  return __builtin_bit_cast(unsigned, r);
}
__device__ __forceinline__ float dot2(unsigned a, unsigned b, float c) {
  return __builtin_amdgcn_fdot2_f32_bf16(__builtin_bit_cast(bf16x2, a), __builtin_bit_cast(bf16x2, b), c, false);
}
template <int CTRL> __device__ __forceinline__ float dpp(float x) {
  return __builtin_bit_cast(float, __builtin_amdgcn_mov_dpp(__builtin_bit_cast(int, x), CTRL, 0xf, 0xf, true));
}
constexpr int XOR1 = 0xB1, XOR2 = 0x4E, XOR7 = 0x141, XOR8 = 0x128;

__device__ __forceinline__ float comb32(float a, float b) {
  auto s = __builtin_amdgcn_permlane32_swap(__float_as_uint(a), __float_as_uint(b), false, false);
  return __uint_as_float(s[0]) + __uint_as_float(s[1]);
}
__device__ __forceinline__ float comb16(float a, float b) {
  auto s = __builtin_amdgcn_permlane16_swap(__float_as_uint(a), __float_as_uint(b), false, false);
  return __uint_as_float(s[0]) + __uint_as_float(s[1]);
}
template <int CTRL> __device__ __forceinline__ float combdpp(float a, float b, bool hi) {
  const float keep = hi ? b : a, send = hi ? a : b;
  return keep + dpp<CTRL>(send);
}
__device__ __forceinline__ float half_max(float x) {
  x = fmaxf(x, dpp<XOR1>(x));
  x = fmaxf(x, dpp<XOR2>(x));
  x = fmaxf(x, dpp<XOR7>(x));
  x = fmaxf(x, dpp<XOR8>(x));
  auto s = __builtin_amdgcn_permlane16_swap(__float_as_uint(x), __float_as_uint(x), false, false);
  return fmaxf(__uint_as_float(s[0]), __uint_as_float(s[1]));
}
__device__ __forceinline__ float half_sum(float x) {
  x += dpp<XOR1>(x);
  x += dpp<XOR2>(x);
  x += dpp<XOR7>(x);
  x += dpp<XOR8>(x);
  auto s = __builtin_amdgcn_permlane16_swap(__float_as_uint(x), __float_as_uint(x), false, false);
  return __uint_as_float(s[0]) + __uint_as_float(s[1]);
}
__device__ __forceinline__ float rdlane(float x, int l) {
  return __builtin_bit_cast(float, __builtin_amdgcn_readlane(__builtin_bit_cast(int, x), l));
}
typedef __amdgpu_buffer_rsrc_t rsrc_t;
__device__ __forceinline__ i32x4 ldrow(rsrc_t r, int voff, int row) {
  return __builtin_amdgcn_raw_buffer_load_b128(r, voff + (row & 3) * 1024, (row >> 2) * 4096, CFG_NT ? 2 : 0);
}
__host__ __device__ constexpr int brev5(int j) {
  return ((j & 1) << 4) | ((j & 2) << 2) | (j & 4) | ((j & 8) >> 2) | ((j & 16) >> 4);
}

constexpr int PREC = DH + 4;

struct WaveState {
  float m, l, c;
  f32x2 a0l, a0h, a1l, a1h;
};

template <bool LAST>
__device__ __forceinline__ void chunk_body(i32x4 (&buf)[W], rsrc_t kp, rsrc_t vp, int voff, int r0,
                                           const float (&kz)[4], const float (&ks)[4], const unsigned (&q0)[2],
                                           const unsigned (&q1)[2], const float (&qa)[4], const float (&qb)[4], float kc0, float kc1,
                                           float vs, float vz, int lane, WaveState& st) {
  float lvl[6];
  float sc = 0.f;
#pragma unroll
  for (int s = 0; s < CH; ++s) {
    __builtin_amdgcn_sched_barrier(0);
    const i32x4 x = buf[s % W];
    {
      const int n = s + W;
      if (n < CH) buf[s % W] = ldrow(kp, voff, r0 + (n));
      else if (n < 2 * CH) buf[s % W] = ldrow(vp, voff, r0 + ((n - CH)));
      else if (!LAST) buf[s % W] = ldrow(kp, voff, r0 + ((n - CH)));
    }
    const float x0 = (float)x[0], x1 = (float)x[1], x2 = (float)x[2], x3 = (float)x[3];
    float d0, d1;
    d0 = fmaf(x3, qa[3], fmaf(x2, qa[2], fmaf(x1, qa[1], fmaf(x0, qa[0], kc0))));
    d1 = fmaf(x3, qb[3], fmaf(x2, qb[2], fmaf(x1, qb[1], fmaf(x0, qb[0], kc1))));
    float v = comb32(d0, d1);
    if ((s & 1) == 0) { lvl[0] = v; continue; }
    v = comb16(lvl[0], v);
    if ((s & 2) == 0) { lvl[1] = v; continue; }
    v = combdpp<XOR8>(lvl[1], v, lane & 8);
    if ((s & 4) == 0) { lvl[2] = v; continue; }
    v = combdpp<XOR7>(lvl[2], v, lane & 4);
    if ((s & 8) == 0) { lvl[3] = v; continue; }
    v = combdpp<XOR2>(lvl[3], v, lane & 2);
    if ((s & 16) == 0) { lvl[4] = v; continue; }
    sc = combdpp<XOR1>(lvl[4], v, lane & 1);
  }
  constexpr float C2 = 0.0625f * 1.4426950408889634f;
  const float xl = sc * C2;
  const float cm = half_max(xl);
  const float mn = fmaxf(st.m, cm);
  const float alpha = __builtin_amdgcn_exp2f(st.m - mn);
  const float p = __builtin_amdgcn_exp2f(xl - mn);
  st.m = mn;
  st.l = fmaf(st.l, alpha, p);
  const float w = p * vs;
  st.c = fmaf(st.c, alpha, w * vz);
  const float a0 = rdlane(alpha, 0), a1 = rdlane(alpha, 32);
  st.a0l *= a0; st.a0h *= a0; st.a1l *= a1; st.a1h *= a1;
#pragma unroll
  for (int j = 0; j < CH; ++j) {
    const int s = CH + j;
    __builtin_amdgcn_sched_barrier(0);
    const i32x4 x = buf[s % W];
    {
      const int n = s + W;
      if (n < 2 * CH) buf[s % W] = ldrow(vp, voff, r0 + ((n - CH)));
      else if (!LAST) {
        if (n < 3 * CH) buf[s % W] = ldrow(kp, voff, r0 + ((n - CH)));
        else buf[s % W] = ldrow(vp, voff, r0 + ((n - 2 * CH)));
      }
    }
    const float w0 = rdlane(w, brev5(j)), w1 = rdlane(w, 32 + brev5(j));
    const f32x2 xl = {(float)x[0], (float)x[1]}, xh = {(float)x[2], (float)x[3]};
    const f32x2 w0v = {w0, w0}, w1v = {w1, w1};
    st.a0l = __builtin_elementwise_fma(w0v, xl, st.a0l);
    st.a0h = __builtin_elementwise_fma(w0v, xh, st.a0h);
    st.a1l = __builtin_elementwise_fma(w1v, xl, st.a1l);
    st.a1h = __builtin_elementwise_fma(w1v, xh, st.a1h);
    asm volatile("" : "+v"(st.a0l), "+v"(st.a0h), "+v"(st.a1l), "+v"(st.a1h));
  }
}

__global__ void __launch_bounds__(WAVES * 64, CFG_MINW)
attn_partial(const unsigned short* __restrict__ q, const int* __restrict__ qk, const int* __restrict__ qv,
             const unsigned short* __restrict__ ksc, const unsigned short* __restrict__ kzr,
             const unsigned short* __restrict__ vsc, const unsigned short* __restrict__ vzr,
             float* __restrict__ part) {
  __shared__ __attribute__((aligned(16))) float lds[WAVES * 2 * PREC];
  const int tid = threadIdx.x, lane = tid & 63;
  const int wave = __builtin_amdgcn_readfirstlane(tid >> 6);
  const int bid = blockIdx.x;
  const int pair = bid / NSPLIT, split = bid % NSPLIT;
  const int t0 = split * TPB + wave * TPW;
  const int grp = t0 / G;
  const size_t rowbase = (size_t)pair * SKV + t0;
  const rsrc_t kp = __builtin_amdgcn_make_buffer_rsrc((void*)(qk + rowbase * DH), 0, TPW * 1024, 0x00020000);
  const rsrc_t vp = __builtin_amdgcn_make_buffer_rsrc((void*)(qv + rowbase * DH), 0, TPW * 1024, 0x00020000);
  const int voff = lane * 16;
  const int r0 = 0;

  const u32x2 qraw0 = *reinterpret_cast<const u32x2*>(q + ((size_t)pair * 2 + 0) * DH + lane * 4);
  const u32x2 qraw1 = *reinterpret_cast<const u32x2*>(q + ((size_t)pair * 2 + 1) * DH + lane * 4);
  const u32x2 ksraw = *reinterpret_cast<const u32x2*>(ksc + ((size_t)pair * NG + grp) * DH + lane * 4);
  const u32x2 kzraw = *reinterpret_cast<const u32x2*>(kzr + ((size_t)pair * NG + grp) * DH + lane * 4);
  const int tl = brev5(lane & 31);
  unsigned short vsr[NC], vzr_[NC];
#pragma unroll
  for (int c = 0; c < NC; ++c) {
    vsr[c] = vsc[rowbase + c * CH + tl];
    vzr_[c] = vzr[rowbase + c * CH + tl];
  }
  i32x4 buf[W];
#pragma unroll
  for (int s = 0; s < W; ++s) {
    __builtin_amdgcn_sched_barrier(0);
    buf[s] = (s < CH) ? ldrow(kp, voff, r0 + (s)) : ldrow(vp, voff, r0 + ((s - CH)));
  }

  const float ks[4] = {bf_lo(ksraw[0]), bf_hi(ksraw[0]), bf_lo(ksraw[1]), bf_hi(ksraw[1])};
  const float kz[4] = {bf_lo(kzraw[0]), bf_hi(kzraw[0]), bf_lo(kzraw[1]), bf_hi(kzraw[1])};
  const unsigned q0[2] = {qraw0[0], qraw0[1]}, q1[2] = {qraw1[0], qraw1[1]};
  float qa[4], qb[4], kc0 = 0.f, kc1 = 0.f;
  {
    const float qf0[4] = {bf_lo(qraw0[0]), bf_hi(qraw0[0]), bf_lo(qraw0[1]), bf_hi(qraw0[1])};
    const float qf1[4] = {bf_lo(qraw1[0]), bf_hi(qraw1[0]), bf_lo(qraw1[1]), bf_hi(qraw1[1])};
#pragma unroll
    for (int d = 0; d < 4; ++d) {
      qa[d] = qf0[d] * ks[d]; qb[d] = qf1[d] * ks[d];
      kc0 = fmaf(-qa[d], kz[d], kc0); kc1 = fmaf(-qb[d], kz[d], kc1);
    }
  }
  WaveState st;
  st.m = -INFINITY; st.l = 0.f; st.c = 0.f;
  st.a0l = st.a0h = st.a1l = st.a1h = f32x2{0.f, 0.f};

#pragma unroll
  for (int c = 0; c < NC; ++c) {
    const float vs = bf_lo(vsr[c]), vz = bf_lo(vzr_[c]);
    if (c + 1 < NC) chunk_body<false>(buf, kp, vp, voff, c * CH, kz, ks, q0, q1, qa, qb, kc0, kc1, vs, vz, lane, st);
    else chunk_body<true>(buf, kp, vp, voff, c * CH, kz, ks, q0, q1, qa, qb, kc0, kc1, vs, vz, lane, st);
  }
  const float L = half_sum(st.l), C = half_sum(st.c);
  const float c0 = rdlane(C, 0), c1 = rdlane(C, 32);
  float* rec0 = lds + (wave * 2 + 0) * PREC;
  float* rec1 = lds + (wave * 2 + 1) * PREC;
  *reinterpret_cast<f32x4*>(rec0 + 4 + lane * 4) = f32x4{st.a0l[0] - c0, st.a0l[1] - c0, st.a0h[0] - c0, st.a0h[1] - c0};
  *reinterpret_cast<f32x4*>(rec1 + 4 + lane * 4) = f32x4{st.a1l[0] - c1, st.a1l[1] - c1, st.a1h[0] - c1, st.a1h[1] - c1};
  if ((lane & 31) == 0) {
    float* rec = lds + (wave * 2 + (lane >> 5)) * PREC;
    rec[0] = st.m; rec[1] = L;
  }
  __syncthreads();
  if (tid < 128) {
    const int h = tid >> 6, d0 = (tid & 63) * 4;
    float M = -INFINITY;
#pragma unroll
    for (int w = 0; w < WAVES; ++w) M = fmaxf(M, lds[(w * 2 + h) * PREC]);
    float Ls = 0.f;
    f32x4 o = {0.f, 0.f, 0.f, 0.f};
#pragma unroll
    for (int w = 0; w < WAVES; ++w) {
      const float* rec = lds + (w * 2 + h) * PREC;
      const float f = __builtin_amdgcn_exp2f(rec[0] - M);
      Ls = fmaf(f, rec[1], Ls);
      const f32x4 a = *reinterpret_cast<const f32x4*>(rec + 4 + d0);
      o += a * f;
    }
    float* dst = part + (((size_t)pair * 2 + h) * NSPLIT + split) * PREC;
    *reinterpret_cast<f32x4*>(dst + 4 + d0) = o;
    if ((tid & 63) == 0) { dst[0] = M; dst[1] = Ls; }
  }
}

__global__ void __launch_bounds__(64) attn_combine(const float* __restrict__ part, unsigned short* __restrict__ out) {
  const int row = blockIdx.x, lane = threadIdx.x;
  const float* base = part + (size_t)row * NSPLIT * PREC;
  float M = -INFINITY;
#pragma unroll
  for (int s = 0; s < NSPLIT; ++s) M = fmaxf(M, base[s * PREC]);
  float L = 0.f;
  f32x4 o = {0.f, 0.f, 0.f, 0.f};
#pragma unroll
  for (int s = 0; s < NSPLIT; ++s) {
    const float f = __builtin_amdgcn_exp2f(base[s * PREC] - M);
    L = fmaf(f, base[s * PREC + 1], L);
    o += *reinterpret_cast<const f32x4*>(base + s * PREC + 4 + lane * 4) * f;
  }
  const float inv = 1.f / L;
  u32x2 r = {cvt_pk(o[0] * inv, o[1] * inv), cvt_pk(o[2] * inv, o[3] * inv)};
  *reinterpret_cast<u32x2*>(out + (size_t)row * DH + lane * 4) = r;
}

}

extern "C" void kernel_launch(void* const* d_in, const int* in_sizes, int n_in, void* d_out, int out_size, void* d_ws,
                              size_t ws_size, hipStream_t stream) {
  const unsigned short* q = (const unsigned short*)d_in[0];
  const int* qk = (const int*)d_in[1];
  const int* qv = (const int*)d_in[2];
  const unsigned short* ks = (const unsigned short*)d_in[3];
  const unsigned short* kz = (const unsigned short*)d_in[4];
  const unsigned short* vs = (const unsigned short*)d_in[5];
  const unsigned short* vz = (const unsigned short*)d_in[6];
  float* part = (float*)d_ws;
  attn_partial<<<dim3(NPAIR * NSPLIT), dim3(WAVES * 64), 0, stream>>>(q, qk, qv, ks, kz, vs, vz, part);
  attn_combine<<<dim3(B * HQ), dim3(64), 0, stream>>>(part, (unsigned short*)d_out);
}
```

```cpp
#include <hip/hip_runtime.h>
#include <hip/hip_bf16.h>
#include <stdint.h>

#define CFG_CH 16
#define CFG_U 4
#define CFG_TPB 512
#define CFG_PAIRSTEP 2
#define CFG_LDSPAD (64 * 1024)

namespace {

constexpr int B = 8, HQ = 16, HKV = 8, SKV = 4096, DH = 256, G = 128, NG = SKV / G, REP = HQ / HKV;
constexpr int WAVES = 4, CH = CFG_CH, U = CFG_U, TPB = CFG_TPB;
constexpr int CT = CH * WAVES;
constexpr int NCH = TPB / CT;
constexpr int NSPLIT = SKV / TPB;
constexpr int NPAIR = B * HKV;
constexpr int PS = CFG_PAIRSTEP;
static_assert(CT <= G && G % CT == 0, "a chunk stays inside one K group");
static_assert(CH == 16 || CH == 32, "chunk");
static_assert(REP == 2, "two query heads per kv head");

typedef int i32x4 __attribute__((ext_vector_type(4)));
typedef unsigned u32x2 __attribute__((ext_vector_type(2)));
typedef float f32x2 __attribute__((ext_vector_type(2)));
typedef float f32x4 __attribute__((ext_vector_type(4)));
typedef __bf16 bf16x2 __attribute__((ext_vector_type(2)));
typedef __amdgpu_buffer_rsrc_t rsrc_t;

__device__ __forceinline__ float bf_lo(unsigned u) { return __builtin_bit_cast(float, u << 16); }
__device__ __forceinline__ float bf_hi(unsigned u) { return __builtin_bit_cast(float, u & 0xffff0000u); }
__device__ __forceinline__ unsigned cvt_pk(f32x2 v) {
  bf16x2 r = __builtin_convertvector(v, bf16x2);
  return __builtin_bit_cast(unsigned, r);
}
__device__ __forceinline__ f32x2 unpk(unsigned u) { return f32x2{bf_lo(u), bf_hi(u)}; }
__device__ __forceinline__ float dot2(unsigned a, unsigned b, float c) {
  return __builtin_amdgcn_fdot2_f32_bf16(__builtin_bit_cast(bf16x2, a), __builtin_bit_cast(bf16x2, b), c, false);
}
template <int CTRL> __device__ __forceinline__ float dpp(float x) {
  return __builtin_bit_cast(float, __builtin_amdgcn_mov_dpp(__builtin_bit_cast(int, x), CTRL, 0xf, 0xf, true));
}
constexpr int XOR1 = 0xB1, XOR2 = 0x4E, XOR7 = 0x141, XOR8 = 0x128;

__device__ __forceinline__ float comb32(float a, float b) {
  auto s = __builtin_amdgcn_permlane32_swap(__float_as_uint(a), __float_as_uint(b), false, false);
  return __uint_as_float(s[0]) + __uint_as_float(s[1]);
}
__device__ __forceinline__ float comb16(float a, float b) {
  auto s = __builtin_amdgcn_permlane16_swap(__float_as_uint(a), __float_as_uint(b), false, false);
  return __uint_as_float(s[0]) + __uint_as_float(s[1]);
}
template <int CTRL> __device__ __forceinline__ float combdpp(float a, float b, bool hi) {
  const float keep = hi ? b : a, send = hi ? a : b;
  return keep + dpp<CTRL>(send);
}
__device__ __forceinline__ float half_max(float x) {
  x = fmaxf(x, dpp<XOR1>(x));
  x = fmaxf(x, dpp<XOR2>(x));
  x = fmaxf(x, dpp<XOR7>(x));
  x = fmaxf(x, dpp<XOR8>(x));
  auto s = __builtin_amdgcn_permlane16_swap(__float_as_uint(x), __float_as_uint(x), false, false);
  return fmaxf(__uint_as_float(s[0]), __uint_as_float(s[1]));
}
__device__ __forceinline__ float half_sum(float x) {
  x += dpp<XOR1>(x);
  x += dpp<XOR2>(x);
  x += dpp<XOR7>(x);
  x += dpp<XOR8>(x);
  auto s = __builtin_amdgcn_permlane16_swap(__float_as_uint(x), __float_as_uint(x), false, false);
  return __uint_as_float(s[0]) + __uint_as_float(s[1]);
}
__device__ __forceinline__ float rdlane(float x, int l) {
  return __builtin_bit_cast(float, __builtin_amdgcn_readlane(__builtin_bit_cast(int, x), l));
}
__host__ __device__ constexpr int roff(int j) { return ((j / U) * U * WAVES + (j % U)) * 1024; }
__device__ __forceinline__ i32x4 ldrow(rsrc_t r, int voff, int sbase, int j) {
  return __builtin_amdgcn_raw_buffer_load_b128(r, voff + (roff(j) & 4095), sbase + (roff(j) & ~4095), 2);
}
__host__ __device__ constexpr int brev5(int j) {
  return ((j & 1) << 4) | ((j & 2) << 2) | (j & 4) | ((j & 8) >> 2) | ((j & 16) >> 4);
}

constexpr int PREC = DH + 4;

__global__ void __launch_bounds__(WAVES * 64)
attn_partial(const unsigned short* __restrict__ q, const int* __restrict__ qk, const int* __restrict__ qv,
             const unsigned short* __restrict__ ksc, const unsigned short* __restrict__ kzr,
             const unsigned short* __restrict__ vsc, const unsigned short* __restrict__ vzr,
             float* __restrict__ part) {
  __shared__ __attribute__((aligned(16))) float lds[(CFG_LDSPAD > WAVES * 2 * PREC * 4 ? CFG_LDSPAD : WAVES * 2 * PREC * 4) / 4];
  const int tid = threadIdx.x, lane = tid & 63;
  const int wave = __builtin_amdgcn_readfirstlane(tid >> 6);
  const int bid = blockIdx.x;
  const int pair = bid / NSPLIT, split = bid % NSPLIT;
  const size_t blockrow = (size_t)bid * TPB;
  const size_t waverow = blockrow + wave * U;
  const int nbytes = (TPB - wave * U) * 1024;
  const rsrc_t kp = __builtin_amdgcn_make_buffer_rsrc((void*)(qk + waverow * DH), 0, nbytes, 0x00020000);
  const rsrc_t vp = __builtin_amdgcn_make_buffer_rsrc((void*)(qv + waverow * DH), 0, nbytes, 0x00020000);
  const int voff = lane * 16;

  const u32x2 q0 = *reinterpret_cast<const u32x2*>(q + ((size_t)pair * 2 + 0) * DH + lane * 4);
  const u32x2 q1 = *reinterpret_cast<const u32x2*>(q + ((size_t)pair * 2 + 1) * DH + lane * 4);
  const size_t gbase = ((size_t)pair * NG + split * (TPB / G)) * DH;
  const unsigned short* ksb = ksc + gbase + lane * 4;
  const unsigned short* kzb = kzr + gbase + lane * 4;
  const int jl = (CH == 32) ? brev5(lane & 31) : (brev5(lane & 31) & 15);
  const int tl = (jl / U) * U * WAVES + (jl % U);
  const unsigned short* vsb = vsc + waverow + tl;
  const unsigned short* vzb = vzr + waverow + tl;
  u32x2 ksn = *reinterpret_cast<const u32x2*>(ksb);
  u32x2 kzn = *reinterpret_cast<const u32x2*>(kzb);
  unsigned short vsn = vsb[0], vzn = vzb[0];

  i32x4 buf[CH];
#pragma unroll
  for (int s = 0; s < CH; ++s) {
    __builtin_amdgcn_sched_barrier(0);
    buf[s] = ldrow(kp, voff, 0, s);
  }
  __builtin_amdgcn_sched_barrier(0);

  float m = -INFINITY, l = 0.f, cc = 0.f;
  f32x2 a0l = {0.f, 0.f}, a0h = a0l, a1l = a0l, a1h = a0l;
  const bool b8 = lane & 8, b4 = lane & 4, b2 = lane & 2, b1 = lane & 1;

#pragma unroll 1
  for (int c = 0; c < NCH; ++c) {
    const int sb = c * CT * 1024;
    const f32x2 ksl = unpk(ksn[0]), ksh = unpk(ksn[1]), kzl = unpk(kzn[0]), kzh = unpk(kzn[1]);
    const float vs = bf_lo(vsn), vz = bf_lo(vzn);
    {
      const int cn = (c + 1 < NCH) ? c + 1 : c;
      const int gn = (cn * CT) / G;
      ksn = *reinterpret_cast<const u32x2*>(ksb + gn * DH);
      kzn = *reinterpret_cast<const u32x2*>(kzb + gn * DH);
      vsn = vsb[cn * CT];
      vzn = vzb[cn * CT];
    }
    float lvl[5];
    float sc = 0.f;
#pragma unroll
    for (int s0 = 0; s0 < CH; s0 += PS) {
      __builtin_amdgcn_sched_barrier(0);
      f32x2 xl[PS], xh[PS];
#pragma unroll
      for (int e = 0; e < PS; ++e) {
        const i32x4 x = buf[s0 + e];
        xl[e] = f32x2{(float)x[0], (float)x[1]};
        xh[e] = f32x2{(float)x[2], (float)x[3]};
        asm volatile("" : "+v"(xl[e]), "+v"(xh[e]));
        buf[s0 + e] = ldrow(vp, voff, sb, s0 + e);
      }
#pragma unroll
      for (int e = 0; e < PS; ++e) {
        const int s = s0 + e;
        const unsigned t01 = cvt_pk(xl[e] - kzl), t23 = cvt_pk(xh[e] - kzh);
        const unsigned k01 = cvt_pk(unpk(t01) * ksl), k23 = cvt_pk(unpk(t23) * ksh);
        const float d0 = dot2(k23, q0[1], dot2(k01, q0[0], 0.f));
        const float d1 = dot2(k23, q1[1], dot2(k01, q1[0], 0.f));
        float v = comb32(d0, d1);
        if ((s & 1) == 0) { lvl[0] = v; continue; }
        v = comb16(lvl[0], v);
        if ((s & 2) == 0) { lvl[1] = v; continue; }
        v = combdpp<XOR8>(lvl[1], v, b8);
        if ((s & 4) == 0) { lvl[2] = v; continue; }
        v = combdpp<XOR7>(lvl[2], v, b4);
        if ((s & 8) == 0) { lvl[3] = v; continue; }
        v = combdpp<XOR2>(lvl[3], v, b2);
        if (CH == 16) { sc = v + dpp<XOR1>(v); continue; }
        if ((s & 16) == 0) { lvl[4] = v; continue; }
        sc = combdpp<XOR1>(lvl[4], v, b1);
      }
    }
    __builtin_amdgcn_sched_barrier(0);
    sc = bf_lo(cvt_pk(f32x2{sc, sc}));
    constexpr float C2 = 0.0625f * 1.4426950408889634f;
    const float xs = sc * C2;
    const float mn = fmaxf(m, half_max(xs));
    const float alpha = __builtin_amdgcn_exp2f(m - mn);
    float p = __builtin_amdgcn_exp2f(xs - mn);
    m = mn;
    const float w = p * vs;
    l = fmaf(l, alpha, p);
    cc = fmaf(cc, alpha, w * vz);
    const float al0 = rdlane(alpha, 0), al1 = rdlane(alpha, 32);
    a0l *= al0; a0h *= al0; a1l *= al1; a1h *= al1;
    const int sbn = sb + CT * 1024;
#pragma unroll
    for (int j0 = 0; j0 < CH; j0 += PS) {
      __builtin_amdgcn_sched_barrier(0);
      f32x2 xl[PS], xh[PS];
#pragma unroll
      for (int e = 0; e < PS; ++e) {
        const i32x4 x = buf[j0 + e];
        xl[e] = f32x2{(float)x[0], (float)x[1]};
        xh[e] = f32x2{(float)x[2], (float)x[3]};
        asm volatile("" : "+v"(xl[e]), "+v"(xh[e]));
        buf[j0 + e] = ldrow(kp, voff, sbn, j0 + e);
      }
#pragma unroll
      for (int e = 0; e < PS; ++e) {
        const int j = j0 + e;
        const float w0 = rdlane(w, brev5(j)), w1 = rdlane(w, 32 + brev5(j));
        const f32x2 w0v = {w0, w0}, w1v = {w1, w1};
        a0l = __builtin_elementwise_fma(w0v, xl[e], a0l);
        a0h = __builtin_elementwise_fma(w0v, xh[e], a0h);
        a1l = __builtin_elementwise_fma(w1v, xl[e], a1l);
        a1h = __builtin_elementwise_fma(w1v, xh[e], a1h);
      }
      asm volatile("" : "+v"(a0l), "+v"(a0h), "+v"(a1l), "+v"(a1h));
    }
  }
  float L = half_sum(l), C = half_sum(cc);
  if (CH == 16) { L *= 0.5f; C *= 0.5f; }
  const float c0 = rdlane(C, 0), c1 = rdlane(C, 32);
  float* rec0 = lds + (wave * 2 + 0) * PREC;
  float* rec1 = lds + (wave * 2 + 1) * PREC;
  *reinterpret_cast<f32x4*>(rec0 + 4 + lane * 4) = f32x4{a0l[0] - c0, a0l[1] - c0, a0h[0] - c0, a0h[1] - c0};
  *reinterpret_cast<f32x4*>(rec1 + 4 + lane * 4) = f32x4{a1l[0] - c1, a1l[1] - c1, a1h[0] - c1, a1h[1] - c1};
  if ((lane & 31) == 0) {
    float* rec = lds + (wave * 2 + (lane >> 5)) * PREC;
    rec[0] = m; rec[1] = L;
  }
  __syncthreads();
  {
    const int h = tid >> 7, d0 = (tid & 127) * 2;
    float M = -INFINITY;
#pragma unroll
    for (int w = 0; w < WAVES; ++w) M = fmaxf(M, lds[(w * 2 + h) * PREC]);
    float Ls = 0.f;
    f32x2 o = {0.f, 0.f};
#pragma unroll
    for (int w = 0; w < WAVES; ++w) {
      const float* rec = lds + (w * 2 + h) * PREC;
      const float f = __builtin_amdgcn_exp2f(rec[0] - M);
      Ls = fmaf(f, rec[1], Ls);
      o += *reinterpret_cast<const f32x2*>(rec + 4 + d0) * f;
    }
    float* dst = part + (((size_t)pair * 2 + h) * NSPLIT + split) * PREC;
    *reinterpret_cast<f32x2*>(dst + 4 + d0) = o;
    if ((tid & 127) == 0) *reinterpret_cast<f32x2*>(dst) = f32x2{M, Ls};
  }
}

__global__ void __launch_bounds__(64) attn_combine(const float* __restrict__ part, unsigned short* __restrict__ out) {
  const int row = blockIdx.x, lane = threadIdx.x;
  const float* base = part + (size_t)row * NSPLIT * PREC;
  f32x2 ml[NSPLIT];
  f32x4 a[NSPLIT];
#pragma unroll
  for (int s = 0; s < NSPLIT; ++s) {
    ml[s] = *reinterpret_cast<const f32x2*>(base + s * PREC);
    a[s] = *reinterpret_cast<const f32x4*>(base + s * PREC + 4 + lane * 4);
  }
  float M = -INFINITY;
#pragma unroll
  for (int s = 0; s < NSPLIT; ++s) M = fmaxf(M, ml[s][0]);
  float L = 0.f;
  f32x4 o = {0.f, 0.f, 0.f, 0.f};
#pragma unroll
  for (int s = 0; s < NSPLIT; ++s) {
    const float f = __builtin_amdgcn_exp2f(ml[s][0] - M);
    L = fmaf(f, ml[s][1], L);
    o += a[s] * f;
  }
  const float inv = 1.f / L;
  u32x2 r = {cvt_pk(f32x2{o[0] * inv, o[1] * inv}), cvt_pk(f32x2{o[2] * inv, o[3] * inv})};
  *reinterpret_cast<u32x2*>(out + (size_t)row * DH + lane * 4) = r;
}

}

extern "C" void kernel_launch(void* const* d_in, const int* in_sizes, int n_in, void* d_out, int out_size, void* d_ws,
                              size_t ws_size, hipStream_t stream) {
  const unsigned short* q = (const unsigned short*)d_in[0];
  const int* qk = (const int*)d_in[1];
  const int* qv = (const int*)d_in[2];
  const unsigned short* ks = (const unsigned short*)d_in[3];
  const unsigned short* kz = (const unsigned short*)d_in[4];
  const unsigned short* vs = (const unsigned short*)d_in[5];
  const unsigned short* vz = (const unsigned short*)d_in[6];
  float* part = (float*)d_ws;
  attn_partial<<<dim3(NPAIR * NSPLIT), dim3(WAVES * 64), 0, stream>>>(q, qk, qv, ks, kz, vs, vz, part);
  attn_combine<<<dim3(B * HQ), dim3(64), 0, stream>>>(part, (unsigned short*)d_out);
}
```

```cpp
#include <hip/hip_runtime.h>
#include <hip/hip_bf16.h>
#include <stdint.h>
#include <stdio.h>

#define CFG_CH 32
#define CFG_U 4
#define CFG_TPB 1024
#define CFG_PAIRSTEP 2
#define CFG_LDSPAD (96 * 1024)

namespace {

constexpr int B = 8, HQ = 16, HKV = 8, SKV = 4096, DH = 256, G = 128, NG = SKV / G, REP = HQ / HKV;
constexpr int WAVES = 4, CH = CFG_CH, U = CFG_U, TPB = CFG_TPB;
constexpr int CT = CH * WAVES;
constexpr int NCH = TPB / CT;
constexpr int NSPLIT = SKV / TPB;
constexpr int NPAIR = B * HKV;
constexpr int PS = CFG_PAIRSTEP;
static_assert(CT <= G && G % CT == 0, "a chunk stays inside one K group");
static_assert(CH == 16 || CH == 32, "chunk");
static_assert(REP == 2, "two query heads per kv head");

typedef int i32x4 __attribute__((ext_vector_type(4)));
typedef unsigned u32x2 __attribute__((ext_vector_type(2)));
typedef float f32x2 __attribute__((ext_vector_type(2)));
typedef float f32x4 __attribute__((ext_vector_type(4)));
typedef __bf16 bf16x2 __attribute__((ext_vector_type(2)));
typedef __amdgpu_buffer_rsrc_t rsrc_t;

__device__ __forceinline__ float bf_lo(unsigned u) { return __builtin_bit_cast(float, u << 16); }
__device__ __forceinline__ float bf_hi(unsigned u) { return __builtin_bit_cast(float, u & 0xffff0000u); }
__device__ __forceinline__ unsigned cvt_pk(f32x2 v) {
  bf16x2 r = __builtin_convertvector(v, bf16x2);
  return __builtin_bit_cast(unsigned, r);
}
__device__ __forceinline__ f32x2 unpk(unsigned u) { return f32x2{bf_lo(u), bf_hi(u)}; }
__device__ __forceinline__ float dot2(unsigned a, unsigned b, float c) {
  return __builtin_amdgcn_fdot2_f32_bf16(__builtin_bit_cast(bf16x2, a), __builtin_bit_cast(bf16x2, b), c, false);
}
template <int CTRL> __device__ __forceinline__ float dpp(float x) {
  return __builtin_bit_cast(float, __builtin_amdgcn_mov_dpp(__builtin_bit_cast(int, x), CTRL, 0xf, 0xf, true));
}
constexpr int XOR1 = 0xB1, XOR2 = 0x4E, XOR7 = 0x141, XOR8 = 0x128;

__device__ __forceinline__ float comb32(float a, float b) {
  auto s = __builtin_amdgcn_permlane32_swap(__float_as_uint(a), __float_as_uint(b), false, false);
  return __uint_as_float(s[0]) + __uint_as_float(s[1]);
}
__device__ __forceinline__ float comb16(float a, float b) {
  auto s = __builtin_amdgcn_permlane16_swap(__float_as_uint(a), __float_as_uint(b), false, false);
  return __uint_as_float(s[0]) + __uint_as_float(s[1]);
}
template <int CTRL> __device__ __forceinline__ float combdpp(float a, float b, bool hi) {
  const float keep = hi ? b : a, send = hi ? a : b;
  return keep + dpp<CTRL>(send);
}
__device__ __forceinline__ float half_max(float x) {
  x = fmaxf(x, dpp<XOR1>(x));
  x = fmaxf(x, dpp<XOR2>(x));
  x = fmaxf(x, dpp<XOR7>(x));
  x = fmaxf(x, dpp<XOR8>(x));
  auto s = __builtin_amdgcn_permlane16_swap(__float_as_uint(x), __float_as_uint(x), false, false);
  return fmaxf(__uint_as_float(s[0]), __uint_as_float(s[1]));
}
__device__ __forceinline__ float half_sum(float x) {
  x += dpp<XOR1>(x);
  x += dpp<XOR2>(x);
  x += dpp<XOR7>(x);
  x += dpp<XOR8>(x);
  auto s = __builtin_amdgcn_permlane16_swap(__float_as_uint(x), __float_as_uint(x), false, false);
  return __uint_as_float(s[0]) + __uint_as_float(s[1]);
}
__device__ __forceinline__ float rdlane(float x, int l) {
  return __builtin_bit_cast(float, __builtin_amdgcn_readlane(__builtin_bit_cast(int, x), l));
}
__host__ __device__ constexpr int roff(int j) { return ((j / U) * U * WAVES + (j % U)) * 1024; }
__device__ __forceinline__ i32x4 ldrow(rsrc_t r, int voff, int sbase, int j) {
  return __builtin_amdgcn_raw_buffer_load_b128(r, voff + (roff(j) & 4095), sbase + (roff(j) & ~4095), 2);
}
__host__ __device__ constexpr int brev5(int j) {
  return ((j & 1) << 4) | ((j & 2) << 2) | (j & 4) | ((j & 8) >> 2) | ((j & 16) >> 4);
}

constexpr int PREC = DH + 4;

__global__ void __launch_bounds__(WAVES * 64)
attn_partial(const unsigned short* __restrict__ q, const int* __restrict__ qk, const int* __restrict__ qv,
             const unsigned short* __restrict__ ksc, const unsigned short* __restrict__ kzr,
             const unsigned short* __restrict__ vsc, const unsigned short* __restrict__ vzr,
             float* __restrict__ part) {
  __shared__ __attribute__((aligned(16))) float lds[(CFG_LDSPAD > WAVES * 2 * PREC * 4 ? CFG_LDSPAD : WAVES * 2 * PREC * 4) / 4];
  const int tid = threadIdx.x, lane = tid & 63;
  const int wave = __builtin_amdgcn_readfirstlane(tid >> 6);
  const int bid = blockIdx.x;
  const int pair = bid / NSPLIT, split = bid % NSPLIT;
  const size_t blockrow = (size_t)bid * TPB;
  const size_t waverow = blockrow + wave * U;
  const int nbytes = (TPB - wave * U) * 1024;
  const rsrc_t kp = __builtin_amdgcn_make_buffer_rsrc((void*)(qk + waverow * DH), 0, nbytes, 0x00020000);
  const rsrc_t vp = __builtin_amdgcn_make_buffer_rsrc((void*)(qv + waverow * DH), 0, nbytes, 0x00020000);
  const int voff = lane * 16;

  const u32x2 q0 = *reinterpret_cast<const u32x2*>(q + ((size_t)pair * 2 + 0) * DH + lane * 4);
  const u32x2 q1 = *reinterpret_cast<const u32x2*>(q + ((size_t)pair * 2 + 1) * DH + lane * 4);
  const size_t gbase = ((size_t)pair * NG + split * (TPB / G)) * DH;
  const unsigned short* ksb = ksc + gbase + lane * 4;
  const unsigned short* kzb = kzr + gbase + lane * 4;
  const int jl = (CH == 32) ? brev5(lane & 31) : (brev5(lane & 31) & 15);
  const int tl = (jl / U) * U * WAVES + (jl % U);
  const unsigned short* vsb = vsc + waverow + tl;
  const unsigned short* vzb = vzr + waverow + tl;
  u32x2 ksn = *reinterpret_cast<const u32x2*>(ksb);
  u32x2 kzn = *reinterpret_cast<const u32x2*>(kzb);
  unsigned short vsn = vsb[0], vzn = vzb[0];

  i32x4 buf[CH];
#pragma unroll
  for (int s = 0; s < CH; ++s) {
    __builtin_amdgcn_sched_barrier(0);
    buf[s] = ldrow(kp, voff, 0, s);
  }
  __builtin_amdgcn_sched_barrier(0);

  float m = -INFINITY, l = 0.f, cc = 0.f;
  f32x2 a0l = {0.f, 0.f}, a0h = a0l, a1l = a0l, a1h = a0l;
  const bool b8 = lane & 8, b4 = lane & 4, b2 = lane & 2, b1 = lane & 1;

#pragma unroll 1
  for (int c = 0; c < NCH; ++c) {
    const int sb = c * CT * 1024;
    const f32x2 ksl = unpk(ksn[0]), ksh = unpk(ksn[1]), kzl = unpk(kzn[0]), kzh = unpk(kzn[1]);
    const float vs = bf_lo(vsn), vz = bf_lo(vzn);
    {
      const int cn = (c + 1 < NCH) ? c + 1 : c;
      const int gn = (cn * CT) / G;
      ksn = *reinterpret_cast<const u32x2*>(ksb + gn * DH);
      kzn = *reinterpret_cast<const u32x2*>(kzb + gn * DH);
      vsn = vsb[cn * CT];
      vzn = vzb[cn * CT];
    }
    float lvl[5];
    float sc = 0.f;
#pragma unroll
    for (int s0 = 0; s0 < CH; s0 += PS) {
      __builtin_amdgcn_sched_barrier(0);
      f32x2 xl[PS], xh[PS];
#pragma unroll
      for (int e = 0; e < PS; ++e) {
        const i32x4 x = buf[s0 + e];
        xl[e] = f32x2{(float)x[0], (float)x[1]};
        xh[e] = f32x2{(float)x[2], (float)x[3]};
        asm volatile("" : "+v"(xl[e]), "+v"(xh[e]));
        buf[s0 + e] = ldrow(vp, voff, sb, s0 + e);
      }
#pragma unroll
      for (int e = 0; e < PS; ++e) {
        const int s = s0 + e;
        const unsigned t01 = cvt_pk(xl[e] - kzl), t23 = cvt_pk(xh[e] - kzh);
        const unsigned k01 = cvt_pk(unpk(t01) * ksl), k23 = cvt_pk(unpk(t23) * ksh);
        const float d0 = dot2(k23, q0[1], dot2(k01, q0[0], 0.f));
        const float d1 = dot2(k23, q1[1], dot2(k01, q1[0], 0.f));
        float v = comb32(d0, d1);
        if ((s & 1) == 0) { lvl[0] = v; continue; }
        v = comb16(lvl[0], v);
        if ((s & 2) == 0) { lvl[1] = v; continue; }
        v = combdpp<XOR8>(lvl[1], v, b8);
        if ((s & 4) == 0) { lvl[2] = v; continue; }
        v = combdpp<XOR7>(lvl[2], v, b4);
        if ((s & 8) == 0) { lvl[3] = v; continue; }
        v = combdpp<XOR2>(lvl[3], v, b2);
        if (CH == 16) { sc = v + dpp<XOR1>(v); continue; }
        if ((s & 16) == 0) { lvl[4] = v; continue; }
        sc = combdpp<XOR1>(lvl[4], v, b1);
      }
    }
    __builtin_amdgcn_sched_barrier(0);
    sc = bf_lo(cvt_pk(f32x2{sc, sc}));
    constexpr float C2 = 0.0625f * 1.4426950408889634f;
    const float xs = sc * C2;
    const float mn = fmaxf(m, half_max(xs));
    const float alpha = __builtin_amdgcn_exp2f(m - mn);
    float p = __builtin_amdgcn_exp2f(xs - mn);
    m = mn;
    const float w = p * vs;
    l = fmaf(l, alpha, p);
    cc = fmaf(cc, alpha, w * vz);
    const float al0 = rdlane(alpha, 0), al1 = rdlane(alpha, 32);
    a0l *= al0; a0h *= al0; a1l *= al1; a1h *= al1;
    const int sbn = sb + CT * 1024;
#pragma unroll
    for (int j0 = 0; j0 < CH; j0 += PS) {
      __builtin_amdgcn_sched_barrier(0);
      f32x2 xl[PS], xh[PS];
#pragma unroll
      for (int e = 0; e < PS; ++e) {
        const i32x4 x = buf[j0 + e];
        xl[e] = f32x2{(float)x[0], (float)x[1]};
        xh[e] = f32x2{(float)x[2], (float)x[3]};
        asm volatile("" : "+v"(xl[e]), "+v"(xh[e]));
        buf[j0 + e] = ldrow(kp, voff, sbn, j0 + e);
      }
#pragma unroll
      for (int e = 0; e < PS; ++e) {
        const int j = j0 + e;
        const float w0 = rdlane(w, brev5(j)), w1 = rdlane(w, 32 + brev5(j));
        const f32x2 w0v = {w0, w0}, w1v = {w1, w1};
        a0l = __builtin_elementwise_fma(w0v, xl[e], a0l);
        a0h = __builtin_elementwise_fma(w0v, xh[e], a0h);
        a1l = __builtin_elementwise_fma(w1v, xl[e], a1l);
        a1h = __builtin_elementwise_fma(w1v, xh[e], a1h);
      }
      asm volatile("" : "+v"(a0l), "+v"(a0h), "+v"(a1l), "+v"(a1h));
    }
  }
  float L = half_sum(l), C = half_sum(cc);
  if (CH == 16) { L *= 0.5f; C *= 0.5f; }
  const float c0 = rdlane(C, 0), c1 = rdlane(C, 32);
  float* rec0 = lds + (wave * 2 + 0) * PREC;
  float* rec1 = lds + (wave * 2 + 1) * PREC;
  *reinterpret_cast<f32x4*>(rec0 + 4 + lane * 4) = f32x4{a0l[0] - c0, a0l[1] - c0, a0h[0] - c0, a0h[1] - c0};
  *reinterpret_cast<f32x4*>(rec1 + 4 + lane * 4) = f32x4{a1l[0] - c1, a1l[1] - c1, a1h[0] - c1, a1h[1] - c1};
  if ((lane & 31) == 0) {
    float* rec = lds + (wave * 2 + (lane >> 5)) * PREC;
    rec[0] = m; rec[1] = L;
  }
  __syncthreads();
  {
    const int h = tid >> 7, d0 = (tid & 127) * 2;
    float M = -INFINITY;
#pragma unroll
    for (int w = 0; w < WAVES; ++w) M = fmaxf(M, lds[(w * 2 + h) * PREC]);
    float Ls = 0.f;
    f32x2 o = {0.f, 0.f};
#pragma unroll
    for (int w = 0; w < WAVES; ++w) {
      const float* rec = lds + (w * 2 + h) * PREC;
      const float f = __builtin_amdgcn_exp2f(rec[0] - M);
      Ls = fmaf(f, rec[1], Ls);
      o += *reinterpret_cast<const f32x2*>(rec + 4 + d0) * f;
    }
    float* dst = part + (((size_t)pair * 2 + h) * NSPLIT + split) * PREC;
    *reinterpret_cast<f32x2*>(dst + 4 + d0) = o;
    if ((tid & 127) == 0) *reinterpret_cast<f32x2*>(dst) = f32x2{M, Ls};
  }
}

__global__ void __launch_bounds__(64) attn_combine(const float* __restrict__ part, unsigned short* __restrict__ out) {
  const int row = blockIdx.x, lane = threadIdx.x;
  const float* base = part + (size_t)row * NSPLIT * PREC;
  f32x2 ml[NSPLIT];
  f32x4 a[NSPLIT];
#pragma unroll
  for (int s = 0; s < NSPLIT; ++s) {
    ml[s] = *reinterpret_cast<const f32x2*>(base + s * PREC);
    a[s] = *reinterpret_cast<const f32x4*>(base + s * PREC + 4 + lane * 4);
  }
  float M = -INFINITY;
#pragma unroll
  for (int s = 0; s < NSPLIT; ++s) M = fmaxf(M, ml[s][0]);
  float L = 0.f;
  f32x4 o = {0.f, 0.f, 0.f, 0.f};
#pragma unroll
  for (int s = 0; s < NSPLIT; ++s) {
    const float f = __builtin_amdgcn_exp2f(ml[s][0] - M);
    L = fmaf(f, ml[s][1], L);
    o += a[s] * f;
  }
  const float inv = 1.f / L;
  u32x2 r = {cvt_pk(f32x2{o[0] * inv, o[1] * inv}), cvt_pk(f32x2{o[2] * inv, o[3] * inv})};
  *reinterpret_cast<u32x2*>(out + (size_t)row * DH + lane * 4) = r;
}

}

extern "C" void kernel_launch(void* const* d_in, const int* in_sizes, int n_in, void* d_out, int out_size, void* d_ws,
                              size_t ws_size, hipStream_t stream) {
  const unsigned short* q = (const unsigned short*)d_in[0];
  const int* qk = (const int*)d_in[1];
  const int* qv = (const int*)d_in[2];
  const unsigned short* ks = (const unsigned short*)d_in[3];
  const unsigned short* kz = (const unsigned short*)d_in[4];
  const unsigned short* vs = (const unsigned short*)d_in[5];
  const unsigned short* vz = (const unsigned short*)d_in[6];
  float* part = (float*)d_ws;
  for (int i = 0; i < 7; ++i) fprintf(stderr, "DBG in[%d]=%p\n", i, d_in[i]);
  fprintf(stderr, "DBG out=%p ws=%p\n", d_out, d_ws);
  attn_partial<<<dim3(NPAIR * NSPLIT), dim3(WAVES * 64), 0, stream>>>(q, qk, qv, ks, kz, vs, vz, part);
  attn_combine<<<dim3(B * HQ), dim3(64), 0, stream>>>(part, (unsigned short*)d_out);
}
```

```cpp
#include <hip/hip_runtime.h>
#include <hip/hip_bf16.h>
#include <stdint.h>

#define CFG_CH 32
#define CFG_U 4
#define CFG_TPB 1024
#define CFG_PAIRSTEP 2
#define CFG_LDSPAD (96 * 1024)

namespace {

constexpr int B = 8, HQ = 16, HKV = 8, SKV = 4096, DH = 256, G = 128, NG = SKV / G, REP = HQ / HKV;
constexpr int WAVES = 4, CH = CFG_CH, U = CFG_U, TPB = CFG_TPB;
constexpr int CT = CH * WAVES;
constexpr int NCH = TPB / CT;
constexpr int NSPLIT = SKV / TPB;
constexpr int NPAIR = B * HKV;
constexpr int PS = CFG_PAIRSTEP;
static_assert(CT <= G && G % CT == 0, "a chunk stays inside one K group");
static_assert(CH == 16 || CH == 32, "chunk");
static_assert(REP == 2, "two query heads per kv head");

typedef int i32x4 __attribute__((ext_vector_type(4)));
typedef unsigned u32x2 __attribute__((ext_vector_type(2)));
typedef float f32x2 __attribute__((ext_vector_type(2)));
typedef float f32x4 __attribute__((ext_vector_type(4)));
typedef __bf16 bf16x2 __attribute__((ext_vector_type(2)));
typedef __amdgpu_buffer_rsrc_t rsrc_t;

__device__ __forceinline__ float bf_lo(unsigned u) { return __builtin_bit_cast(float, u << 16); }
__device__ __forceinline__ float bf_hi(unsigned u) { return __builtin_bit_cast(float, u & 0xffff0000u); }
__device__ __forceinline__ unsigned cvt_pk(f32x2 v) {
  bf16x2 r = __builtin_convertvector(v, bf16x2);
  return __builtin_bit_cast(unsigned, r);
}
__device__ __forceinline__ f32x2 unpk(unsigned u) { return f32x2{bf_lo(u), bf_hi(u)}; }
__device__ __forceinline__ float dot2(unsigned a, unsigned b, float c) {
  return __builtin_amdgcn_fdot2_f32_bf16(__builtin_bit_cast(bf16x2, a), __builtin_bit_cast(bf16x2, b), c, false);
}
template <int CTRL> __device__ __forceinline__ float dpp(float x) {
  return __builtin_bit_cast(float, __builtin_amdgcn_mov_dpp(__builtin_bit_cast(int, x), CTRL, 0xf, 0xf, true));
}
constexpr int XOR1 = 0xB1, XOR2 = 0x4E, XOR7 = 0x141, XOR8 = 0x128;

__device__ __forceinline__ float comb32(float a, float b) {
  auto s = __builtin_amdgcn_permlane32_swap(__float_as_uint(a), __float_as_uint(b), false, false);
  return __uint_as_float(s[0]) + __uint_as_float(s[1]);
}
__device__ __forceinline__ float comb16(float a, float b) {
  auto s = __builtin_amdgcn_permlane16_swap(__float_as_uint(a), __float_as_uint(b), false, false);
  return __uint_as_float(s[0]) + __uint_as_float(s[1]);
}
template <int CTRL> __device__ __forceinline__ float combdpp(float a, float b, bool hi) {
  const float keep = hi ? b : a, send = hi ? a : b;
  return keep + dpp<CTRL>(send);
}
__device__ __forceinline__ float half_max(float x) {
  x = fmaxf(x, dpp<XOR1>(x));
  x = fmaxf(x, dpp<XOR2>(x));
  x = fmaxf(x, dpp<XOR7>(x));
  x = fmaxf(x, dpp<XOR8>(x));
  auto s = __builtin_amdgcn_permlane16_swap(__float_as_uint(x), __float_as_uint(x), false, false);
  return fmaxf(__uint_as_float(s[0]), __uint_as_float(s[1]));
}
__device__ __forceinline__ float half_sum(float x) {
  x += dpp<XOR1>(x);
  x += dpp<XOR2>(x);
  x += dpp<XOR7>(x);
  x += dpp<XOR8>(x);
  auto s = __builtin_amdgcn_permlane16_swap(__float_as_uint(x), __float_as_uint(x), false, false);
  return __uint_as_float(s[0]) + __uint_as_float(s[1]);
}
__device__ __forceinline__ float rdlane(float x, int l) {
  return __builtin_bit_cast(float, __builtin_amdgcn_readlane(__builtin_bit_cast(int, x), l));
}
__host__ __device__ constexpr int roff(int j) { return ((j / U) * U * WAVES + (j % U)) * 1024; }
#define AUXK 2
#define AUXV 2
template <int AUX> __device__ __forceinline__ i32x4 ldrow(rsrc_t r, int voff, int sbase, int j) {
  return __builtin_amdgcn_raw_buffer_load_b128(r, voff + (roff(j) & 4095), sbase + (roff(j) & ~4095), AUX);
}
__host__ __device__ constexpr int brev5(int j) {
  return ((j & 1) << 4) | ((j & 2) << 2) | (j & 4) | ((j & 8) >> 2) | ((j & 16) >> 4);
}

constexpr int PREC = DH + 4;

__global__ void __launch_bounds__(WAVES * 64)
attn_partial(const unsigned short* __restrict__ q, const int* __restrict__ qk, const int* __restrict__ qv,
             const unsigned short* __restrict__ ksc, const unsigned short* __restrict__ kzr,
             const unsigned short* __restrict__ vsc, const unsigned short* __restrict__ vzr,
             float* __restrict__ part) {
  __shared__ __attribute__((aligned(16))) float lds[(CFG_LDSPAD > WAVES * 2 * PREC * 4 ? CFG_LDSPAD : WAVES * 2 * PREC * 4) / 4];
  const int tid = threadIdx.x, lane = tid & 63;
  const int wave = __builtin_amdgcn_readfirstlane(tid >> 6);
  const int bid = blockIdx.x;
  const int pair = bid / NSPLIT, split = bid % NSPLIT;
  const size_t blockrow = (size_t)bid * TPB;
  const size_t waverow = blockrow + wave * U;
  const int nbytes = (TPB - wave * U) * 1024;
  const rsrc_t kp = __builtin_amdgcn_make_buffer_rsrc((void*)(qk + waverow * DH), 0, nbytes, 0x00020000);
  const rsrc_t vp = __builtin_amdgcn_make_buffer_rsrc((void*)(qv + waverow * DH), 0, nbytes, 0x00020000);
  const int voff = lane * 16;

  const u32x2 q0 = *reinterpret_cast<const u32x2*>(q + ((size_t)pair * 2 + 0) * DH + lane * 4);
  const u32x2 q1 = *reinterpret_cast<const u32x2*>(q + ((size_t)pair * 2 + 1) * DH + lane * 4);
  const size_t gbase = ((size_t)pair * NG + split * (TPB / G)) * DH;
  const unsigned short* ksb = ksc + gbase + lane * 4;
  const unsigned short* kzb = kzr + gbase + lane * 4;
  const int jl = (CH == 32) ? brev5(lane & 31) : (brev5(lane & 31) & 15);
  const int tl = (jl / U) * U * WAVES + (jl % U);
  const unsigned short* vsb = vsc + waverow + tl;
  const unsigned short* vzb = vzr + waverow + tl;
  u32x2 ksn = *reinterpret_cast<const u32x2*>(ksb);
  u32x2 kzn = *reinterpret_cast<const u32x2*>(kzb);
  unsigned short vsn = vsb[0], vzn = vzb[0];

  i32x4 buf[CH];
#pragma unroll
  for (int s = 0; s < CH; ++s) {
    __builtin_amdgcn_sched_barrier(0);
    buf[s] = ldrow<AUXK>(kp, voff, 0, s);
  }
  __builtin_amdgcn_sched_barrier(0);

  float m = -INFINITY, l = 0.f, cc = 0.f;
  f32x2 a0l = {0.f, 0.f}, a0h = a0l, a1l = a0l, a1h = a0l;
  const bool b8 = lane & 8, b4 = lane & 4, b2 = lane & 2, b1 = lane & 1;

#pragma unroll 1
  for (int c = 0; c < NCH; ++c) {
    const int sb = c * CT * 1024;
    const f32x2 ksl = unpk(ksn[0]), ksh = unpk(ksn[1]), kzl = unpk(kzn[0]), kzh = unpk(kzn[1]);
    const float vs = bf_lo(vsn), vz = bf_lo(vzn);
    {
      const int cn = (c + 1 < NCH) ? c + 1 : c;
      const int gn = (cn * CT) / G;
      ksn = *reinterpret_cast<const u32x2*>(ksb + gn * DH);
      kzn = *reinterpret_cast<const u32x2*>(kzb + gn * DH);
      vsn = vsb[cn * CT];
      vzn = vzb[cn * CT];
    }
    float lvl[5];
    float sc = 0.f;
#pragma unroll
    for (int s0 = 0; s0 < CH; s0 += PS) {
      __builtin_amdgcn_sched_barrier(0);
      f32x2 xl[PS], xh[PS];
#pragma unroll
      for (int e = 0; e < PS; ++e) {
        const i32x4 x = buf[s0 + e];
        xl[e] = f32x2{(float)x[0], (float)x[1]};
        xh[e] = f32x2{(float)x[2], (float)x[3]};
        asm volatile("" : "+v"(xl[e]), "+v"(xh[e]));
        buf[s0 + e] = ldrow<AUXV>(vp, voff, sb, s0 + e);
      }
#pragma unroll
      for (int e = 0; e < PS; ++e) {
        const int s = s0 + e;
        const unsigned t01 = cvt_pk(xl[e] - kzl), t23 = cvt_pk(xh[e] - kzh);
        const unsigned k01 = cvt_pk(unpk(t01) * ksl), k23 = cvt_pk(unpk(t23) * ksh);
        const float d0 = dot2(k23, q0[1], dot2(k01, q0[0], 0.f));
        const float d1 = dot2(k23, q1[1], dot2(k01, q1[0], 0.f));
        float v = comb32(d0, d1);
        if ((s & 1) == 0) { lvl[0] = v; continue; }
        v = comb16(lvl[0], v);
        if ((s & 2) == 0) { lvl[1] = v; continue; }
        v = combdpp<XOR8>(lvl[1], v, b8);
        if ((s & 4) == 0) { lvl[2] = v; continue; }
        v = combdpp<XOR7>(lvl[2], v, b4);
        if ((s & 8) == 0) { lvl[3] = v; continue; }
        v = combdpp<XOR2>(lvl[3], v, b2);
        if (CH == 16) { sc = v + dpp<XOR1>(v); continue; }
        if ((s & 16) == 0) { lvl[4] = v; continue; }
        sc = combdpp<XOR1>(lvl[4], v, b1);
      }
    }
    __builtin_amdgcn_sched_barrier(0);
    sc = bf_lo(cvt_pk(f32x2{sc, sc}));
    constexpr float C2 = 0.0625f * 1.4426950408889634f;
    const float xs = sc * C2;
    const float mn = fmaxf(m, half_max(xs));
    const float alpha = __builtin_amdgcn_exp2f(m - mn);
    float p = __builtin_amdgcn_exp2f(xs - mn);
    m = mn;
    const float w = p * vs;
    l = fmaf(l, alpha, p);
    cc = fmaf(cc, alpha, w * vz);
    const float al0 = rdlane(alpha, 0), al1 = rdlane(alpha, 32);
    a0l *= al0; a0h *= al0; a1l *= al1; a1h *= al1;
    const int sbn = sb + CT * 1024;
#pragma unroll
    for (int j0 = 0; j0 < CH; j0 += PS) {
      __builtin_amdgcn_sched_barrier(0);
      f32x2 xl[PS], xh[PS];
#pragma unroll
      for (int e = 0; e < PS; ++e) {
        const i32x4 x = buf[j0 + e];
        xl[e] = f32x2{(float)x[0], (float)x[1]};
        xh[e] = f32x2{(float)x[2], (float)x[3]};
        asm volatile("" : "+v"(xl[e]), "+v"(xh[e]));
        buf[j0 + e] = ldrow<AUXK>(kp, voff, sbn, j0 + e);
      }
#pragma unroll
      for (int e = 0; e < PS; ++e) {
        const int j = j0 + e;
        const float w0 = rdlane(w, brev5(j)), w1 = rdlane(w, 32 + brev5(j));
        const f32x2 w0v = {w0, w0}, w1v = {w1, w1};
        a0l = __builtin_elementwise_fma(w0v, xl[e], a0l);
        a0h = __builtin_elementwise_fma(w0v, xh[e], a0h);
        a1l = __builtin_elementwise_fma(w1v, xl[e], a1l);
        a1h = __builtin_elementwise_fma(w1v, xh[e], a1h);
      }
      asm volatile("" : "+v"(a0l), "+v"(a0h), "+v"(a1l), "+v"(a1h));
    }
  }
  float L = half_sum(l), C = half_sum(cc);
  if (CH == 16) { L *= 0.5f; C *= 0.5f; }
  const float c0 = rdlane(C, 0), c1 = rdlane(C, 32);
  float* rec0 = lds + (wave * 2 + 0) * PREC;
  float* rec1 = lds + (wave * 2 + 1) * PREC;
  *reinterpret_cast<f32x4*>(rec0 + 4 + lane * 4) = f32x4{a0l[0] - c0, a0l[1] - c0, a0h[0] - c0, a0h[1] - c0};
  *reinterpret_cast<f32x4*>(rec1 + 4 + lane * 4) = f32x4{a1l[0] - c1, a1l[1] - c1, a1h[0] - c1, a1h[1] - c1};
  if ((lane & 31) == 0) {
    float* rec = lds + (wave * 2 + (lane >> 5)) * PREC;
    rec[0] = m; rec[1] = L;
  }
  __syncthreads();
  {
    const int h = tid >> 7, d0 = (tid & 127) * 2;
    float M = -INFINITY;
#pragma unroll
    for (int w = 0; w < WAVES; ++w) M = fmaxf(M, lds[(w * 2 + h) * PREC]);
    float Ls = 0.f;
    f32x2 o = {0.f, 0.f};
#pragma unroll
    for (int w = 0; w < WAVES; ++w) {
      const float* rec = lds + (w * 2 + h) * PREC;
      const float f = __builtin_amdgcn_exp2f(rec[0] - M);
      Ls = fmaf(f, rec[1], Ls);
      o += *reinterpret_cast<const f32x2*>(rec + 4 + d0) * f;
    }
    float* dst = part + (((size_t)pair * 2 + h) * NSPLIT + split) * PREC;
    *reinterpret_cast<f32x2*>(dst + 4 + d0) = o;
    if ((tid & 127) == 0) *reinterpret_cast<f32x2*>(dst) = f32x2{M, Ls};
  }
}

__global__ void __launch_bounds__(64) attn_combine(const float* __restrict__ part, unsigned short* __restrict__ out) {
  const int row = blockIdx.x, lane = threadIdx.x;
  const float* base = part + (size_t)row * NSPLIT * PREC;
  f32x2 ml[NSPLIT];
  f32x4 a[NSPLIT];
#pragma unroll
  for (int s = 0; s < NSPLIT; ++s) {
    ml[s] = *reinterpret_cast<const f32x2*>(base + s * PREC);
    a[s] = *reinterpret_cast<const f32x4*>(base + s * PREC + 4 + lane * 4);
  }
  float M = -INFINITY;
#pragma unroll
  for (int s = 0; s < NSPLIT; ++s) M = fmaxf(M, ml[s][0]);
  float L = 0.f;
  f32x4 o = {0.f, 0.f, 0.f, 0.f};
#pragma unroll
  for (int s = 0; s < NSPLIT; ++s) {
    const float f = __builtin_amdgcn_exp2f(ml[s][0] - M);
    L = fmaf(f, ml[s][1], L);
    o += a[s] * f;
  }
  const float inv = 1.f / L;
  u32x2 r = {cvt_pk(f32x2{o[0] * inv, o[1] * inv}), cvt_pk(f32x2{o[2] * inv, o[3] * inv})};
  *reinterpret_cast<u32x2*>(out + (size_t)row * DH + lane * 4) = r;
}

}

extern "C" void kernel_launch(void* const* d_in, const int* in_sizes, int n_in, void* d_out, int out_size, void* d_ws,
                              size_t ws_size, hipStream_t stream) {
  const unsigned short* q = (const unsigned short*)d_in[0];
  const int* qk = (const int*)d_in[1];
  const int* qv = (const int*)d_in[2];
  const unsigned short* ks = (const unsigned short*)d_in[3];
  const unsigned short* kz = (const unsigned short*)d_in[4];
  const unsigned short* vs = (const unsigned short*)d_in[5];
  const unsigned short* vz = (const unsigned short*)d_in[6];
  float* part = (float*)d_ws;
  attn_partial<<<dim3(NPAIR * NSPLIT), dim3(WAVES * 64), 0, stream>>>(q, qk, qv, ks, kz, vs, vz, part);
  attn_combine<<<dim3(B * HQ), dim3(64), 0, stream>>>(part, (unsigned short*)d_out);
}
```

```cpp
#include <hip/hip_runtime.h>
#include <hip/hip_bf16.h>
#include <stdint.h>

#define CFG_CH 32
#define CFG_U 4
#define CFG_PAIRSTEP 2
#define CFG_LDSPAD (96 * 1024)
#define CFG_CONS 3
#define CFG_PREF 0
#define CFG_SLEEP 8
#define CFG_CLEAN 1
#define CFG_SKIPVALID 1
#define AUXK 2
#define AUXV 2

namespace {

constexpr int B = 8, HQ = 16, HKV = 8, SKV = 4096, DH = 256, G = 128, NG = SKV / G, REP = HQ / HKV;
constexpr int WAVES = 4, CH = CFG_CH, U = CFG_U, TPB = 1024;
constexpr int CT = CH * WAVES;
constexpr int NCH = TPB / CT;
constexpr int NSPLIT = SKV / TPB;
constexpr int NPAIR = B * HKV;
constexpr int PS = CFG_PAIRSTEP;
constexpr int CONS = CFG_CONS;
static_assert(CT <= G && G % CT == 0, "a chunk stays inside one K group");
static_assert(CH == 16 || CH == 32, "chunk");
static_assert(REP == 2 && NSPLIT == 4, "two query heads per kv head, four splits");

typedef int i32x4 __attribute__((ext_vector_type(4)));
typedef unsigned u32x2 __attribute__((ext_vector_type(2)));
typedef float f32x2 __attribute__((ext_vector_type(2)));
typedef float f32x4 __attribute__((ext_vector_type(4)));
typedef __bf16 bf16x2 __attribute__((ext_vector_type(2)));
typedef __amdgpu_buffer_rsrc_t rsrc_t;
typedef unsigned long long u64;
typedef __attribute__((address_space(1))) u64 gu64;
typedef __attribute__((address_space(1))) unsigned gu32;

__device__ __forceinline__ float bf_lo(unsigned u) { return __builtin_bit_cast(float, u << 16); }
__device__ __forceinline__ float bf_hi(unsigned u) { return __builtin_bit_cast(float, u & 0xffff0000u); }
__device__ __forceinline__ unsigned cvt_pk(f32x2 v) {
  bf16x2 r = __builtin_convertvector(v, bf16x2);
  return __builtin_bit_cast(unsigned, r);
}
__device__ __forceinline__ f32x2 unpk(unsigned u) { return f32x2{bf_lo(u), bf_hi(u)}; }
__device__ __forceinline__ float dot2(unsigned a, unsigned b, float c) {
  return __builtin_amdgcn_fdot2_f32_bf16(__builtin_bit_cast(bf16x2, a), __builtin_bit_cast(bf16x2, b), c, false);
}
template <int CTRL> __device__ __forceinline__ float dpp(float x) {
  return __builtin_bit_cast(float, __builtin_amdgcn_mov_dpp(__builtin_bit_cast(int, x), CTRL, 0xf, 0xf, true));
}
constexpr int XOR1 = 0xB1, XOR2 = 0x4E, XOR7 = 0x141, XOR8 = 0x128;

__device__ __forceinline__ float comb32(float a, float b) {
  auto s = __builtin_amdgcn_permlane32_swap(__float_as_uint(a), __float_as_uint(b), false, false);
  return __uint_as_float(s[0]) + __uint_as_float(s[1]);
}
__device__ __forceinline__ float comb16(float a, float b) {
  auto s = __builtin_amdgcn_permlane16_swap(__float_as_uint(a), __float_as_uint(b), false, false);
  return __uint_as_float(s[0]) + __uint_as_float(s[1]);
}
template <int CTRL> __device__ __forceinline__ float combdpp(float a, float b, bool hi) {
  const float keep = hi ? b : a, send = hi ? a : b;
  return keep + dpp<CTRL>(send);
}
__device__ __forceinline__ float half_max(float x) {
  x = fmaxf(x, dpp<XOR1>(x));
  x = fmaxf(x, dpp<XOR2>(x));
  x = fmaxf(x, dpp<XOR7>(x));
  x = fmaxf(x, dpp<XOR8>(x));
  auto s = __builtin_amdgcn_permlane16_swap(__float_as_uint(x), __float_as_uint(x), false, false);
  return fmaxf(__uint_as_float(s[0]), __uint_as_float(s[1]));
}
__device__ __forceinline__ float half_sum(float x) {
  x += dpp<XOR1>(x);
  x += dpp<XOR2>(x);
  x += dpp<XOR7>(x);
  x += dpp<XOR8>(x);
  auto s = __builtin_amdgcn_permlane16_swap(__float_as_uint(x), __float_as_uint(x), false, false);
  return __uint_as_float(s[0]) + __uint_as_float(s[1]);
}
__device__ __forceinline__ float rdlane(float x, int l) {
  return __builtin_bit_cast(float, __builtin_amdgcn_readlane(__builtin_bit_cast(int, x), l));
}
__host__ __device__ constexpr int roff(int j) { return ((j / U) * U * WAVES + (j % U)) * 1024; }
template <int AUX> __device__ __forceinline__ i32x4 ldrow(rsrc_t r, int voff, int sbase, int j) {
  return __builtin_amdgcn_raw_buffer_load_b128(r, voff + (roff(j) & 4095), sbase + (roff(j) & ~4095), AUX);
}
__host__ __device__ constexpr int brev5(int j) {
  return ((j & 1) << 4) | ((j & 2) << 2) | (j & 4) | ((j & 8) >> 2) | ((j & 16) >> 4);
}
__device__ __forceinline__ void st_gran(gu64* p, unsigned tag, float v) {
  __hip_atomic_store(p, ((u64)tag << 32) | (u64)__float_as_uint(v), __ATOMIC_RELAXED, __HIP_MEMORY_SCOPE_AGENT);
}
__device__ __forceinline__ u64 ld_gran(gu64* p) {
  return __hip_atomic_load(p, __ATOMIC_RELAXED, __HIP_MEMORY_SCOPE_AGENT);
}

constexpr int PREC = DH + 4;
constexpr int GREC = 272;
constexpr size_t WS_GRAN = 16384;

__global__ void __launch_bounds__(WAVES * 64)
attn_fused(const unsigned short* __restrict__ q, const int* __restrict__ qk, const int* __restrict__ qv,
           const unsigned short* __restrict__ ksc, const unsigned short* __restrict__ kzr,
           const unsigned short* __restrict__ vsc, const unsigned short* __restrict__ vzr,
           unsigned char* __restrict__ ws, unsigned short* __restrict__ out) {
  __shared__ __attribute__((aligned(16))) float lds[(CFG_LDSPAD > WAVES * 2 * PREC * 4 ? CFG_LDSPAD : WAVES * 2 * PREC * 4) / 4];
  const int tid = threadIdx.x, lane = tid & 63;
  const int wave = __builtin_amdgcn_readfirstlane(tid >> 6);
  const int bid = blockIdx.x;
#define STAMP(i)
  STAMP(0);
  const int pair = bid / NSPLIT, split = bid % NSPLIT;
  const size_t blockrow = (size_t)bid * TPB;
  const size_t waverow = blockrow + wave * U;
  const int nbytes = (TPB - wave * U) * 1024;
  const rsrc_t kp = __builtin_amdgcn_make_buffer_rsrc((void*)(qk + waverow * DH), 0, nbytes, 0x00020000);
  const rsrc_t vp = __builtin_amdgcn_make_buffer_rsrc((void*)(qv + waverow * DH), 0, nbytes, 0x00020000);
  const int voff = lane * 16;

  const u32x2 q0 = *reinterpret_cast<const u32x2*>(q + ((size_t)pair * 2 + 0) * DH + lane * 4);
  const u32x2 q1 = *reinterpret_cast<const u32x2*>(q + ((size_t)pair * 2 + 1) * DH + lane * 4);
  const size_t gbase = ((size_t)pair * NG + split * (TPB / G)) * DH;
  const unsigned short* ksb = ksc + gbase + lane * 4;
  const unsigned short* kzb = kzr + gbase + lane * 4;
  const int jl = (CH == 32) ? brev5(lane & 31) : (brev5(lane & 31) & 15);
  const int tl = (jl / U) * U * WAVES + (jl % U);
  const unsigned short* vsb = vsc + waverow + tl;
  const unsigned short* vzb = vzr + waverow + tl;
  u32x2 ksn = *reinterpret_cast<const u32x2*>(ksb);
  u32x2 kzn = *reinterpret_cast<const u32x2*>(kzb);
  unsigned short vsn = vsb[0], vzn = vzb[0];

  i32x4 buf[CH];
#pragma unroll
  for (int s = 0; s < CH; ++s) {
    __builtin_amdgcn_sched_barrier(0);
    buf[s] = ldrow<AUXK>(kp, voff, 0, s);
  }
  __builtin_amdgcn_sched_barrier(0);

  gu32* ep = (gu32*)(ws + (size_t)pair * 128);
  const unsigned epoch = __hip_atomic_load(ep, __ATOMIC_RELAXED, __HIP_MEMORY_SCOPE_AGENT);
  const int oh = tid >> 7, oi = tid & 127, od = oi * 2;
  gu64* grec = (gu64*)(ws + WS_GRAN) + ((size_t)pair * 3 * 2 + oh) * GREC;
  const bool is_cons = (split == CONS);
  const int slot = split < CONS ? split : split - 1;
  if (CFG_CLEAN && !is_cons) {
    gu64* r = grec + slot * 2 * GREC;
    st_gran(r + oi, 0u, 0.f);
    st_gran(r + 128 + oi, 0u, 0.f);
    if (oi < 16) st_gran(r + 256 + oi, 0u, 0.f);
  }
  __builtin_amdgcn_sched_barrier(0);

  float m = -INFINITY, l = 0.f, cc = 0.f;
  f32x2 a0l = {0.f, 0.f}, a0h = a0l, a1l = a0l, a1h = a0l;
  const bool b8 = lane & 8, b4 = lane & 4, b2 = lane & 2, b1 = lane & 1;
  u64 g[3][4];
#pragma unroll
  for (int p = 0; p < 3; ++p)
#pragma unroll
    for (int e = 0; e < 4; ++e) g[p][e] = 0;

#pragma unroll 1
  for (int c = 0; c < NCH; ++c) {
    const int sb = c * CT * 1024;
    const f32x2 ksl = unpk(ksn[0]), ksh = unpk(ksn[1]), kzl = unpk(kzn[0]), kzh = unpk(kzn[1]);
    const float vs = bf_lo(vsn), vz = bf_lo(vzn);
    {
      const int cn = (c + 1 < NCH) ? c + 1 : c;
      const int gn = (cn * CT) / G;
      ksn = *reinterpret_cast<const u32x2*>(ksb + gn * DH);
      kzn = *reinterpret_cast<const u32x2*>(kzb + gn * DH);
      vsn = vsb[cn * CT];
      vzn = vzb[cn * CT];
    }
    float lvl[5];
    float sc = 0.f;
#pragma unroll
    for (int s0 = 0; s0 < CH; s0 += PS) {
      __builtin_amdgcn_sched_barrier(0);
      f32x2 xl[PS], xh[PS];
#pragma unroll
      for (int e = 0; e < PS; ++e) {
        const i32x4 x = buf[s0 + e];
        xl[e] = f32x2{(float)x[0], (float)x[1]};
        xh[e] = f32x2{(float)x[2], (float)x[3]};
        asm volatile("" : "+v"(xl[e]), "+v"(xh[e]));
        buf[s0 + e] = ldrow<AUXV>(vp, voff, sb, s0 + e);
      }
      if (s0 == 0) STAMP(1 + 2 * c);
#pragma unroll
      for (int e = 0; e < PS; ++e) {
        const int s = s0 + e;
        const unsigned t01 = cvt_pk(xl[e] - kzl), t23 = cvt_pk(xh[e] - kzh);
        const unsigned k01 = cvt_pk(unpk(t01) * ksl), k23 = cvt_pk(unpk(t23) * ksh);
        const float d0 = dot2(k23, q0[1], dot2(k01, q0[0], 0.f));
        const float d1 = dot2(k23, q1[1], dot2(k01, q1[0], 0.f));
        float v = comb32(d0, d1);
        if ((s & 1) == 0) { lvl[0] = v; continue; }
        v = comb16(lvl[0], v);
        if ((s & 2) == 0) { lvl[1] = v; continue; }
        v = combdpp<XOR8>(lvl[1], v, b8);
        if ((s & 4) == 0) { lvl[2] = v; continue; }
        v = combdpp<XOR7>(lvl[2], v, b4);
        if ((s & 8) == 0) { lvl[3] = v; continue; }
        v = combdpp<XOR2>(lvl[3], v, b2);
        if (CH == 16) { sc = v + dpp<XOR1>(v); continue; }
        if ((s & 16) == 0) { lvl[4] = v; continue; }
        sc = combdpp<XOR1>(lvl[4], v, b1);
      }
    }
    __builtin_amdgcn_sched_barrier(0);
    sc = bf_lo(cvt_pk(f32x2{sc, sc}));
    constexpr float C2 = 0.0625f * 1.4426950408889634f;
    const float xs = sc * C2;
    const float mn = fmaxf(m, half_max(xs));
    const float alpha = __builtin_amdgcn_exp2f(m - mn);
    float p = __builtin_amdgcn_exp2f(xs - mn);
    m = mn;
    const float w = p * vs;
    l = fmaf(l, alpha, p);
    cc = fmaf(cc, alpha, w * vz);
    const float al0 = rdlane(alpha, 0), al1 = rdlane(alpha, 32);
    a0l *= al0; a0h *= al0; a1l *= al1; a1h *= al1;
    const int sbn = sb + CT * 1024;
#pragma unroll
    for (int j0 = 0; j0 < CH; j0 += PS) {
      __builtin_amdgcn_sched_barrier(0);
      f32x2 xl[PS], xh[PS];
#pragma unroll
      for (int e = 0; e < PS; ++e) {
        const i32x4 x = buf[j0 + e];
        xl[e] = f32x2{(float)x[0], (float)x[1]};
        xh[e] = f32x2{(float)x[2], (float)x[3]};
        asm volatile("" : "+v"(xl[e]), "+v"(xh[e]));
        buf[j0 + e] = ldrow<AUXK>(kp, voff, sbn, j0 + e);
      }
      if (j0 == 0) STAMP(2 + 2 * c);
#pragma unroll
      for (int e = 0; e < PS; ++e) {
        const int j = j0 + e;
        const float w0 = rdlane(w, brev5(j)), w1 = rdlane(w, 32 + brev5(j));
        const f32x2 w0v = {w0, w0}, w1v = {w1, w1};
        a0l = __builtin_elementwise_fma(w0v, xl[e], a0l);
        a0h = __builtin_elementwise_fma(w0v, xh[e], a0h);
        a1l = __builtin_elementwise_fma(w1v, xl[e], a1l);
        a1h = __builtin_elementwise_fma(w1v, xh[e], a1h);
      }
      asm volatile("" : "+v"(a0l), "+v"(a0h), "+v"(a1l), "+v"(a1h));
    }
  }
  STAMP(1 + 2 * NCH);
  float L = half_sum(l), C = half_sum(cc);
  if (CH == 16) { L *= 0.5f; C *= 0.5f; }
  const float c0 = rdlane(C, 0), c1 = rdlane(C, 32);
  float* rec0 = lds + (wave * 2 + 0) * PREC;
  float* rec1 = lds + (wave * 2 + 1) * PREC;
  *reinterpret_cast<f32x4*>(rec0 + 4 + lane * 4) = f32x4{a0l[0] - c0, a0l[1] - c0, a0h[0] - c0, a0h[1] - c0};
  *reinterpret_cast<f32x4*>(rec1 + 4 + lane * 4) = f32x4{a1l[0] - c1, a1l[1] - c1, a1h[0] - c1, a1h[1] - c1};
  if ((lane & 31) == 0) {
    float* rec = lds + (wave * 2 + (lane >> 5)) * PREC;
    rec[0] = m; rec[1] = L;
  }
  __syncthreads();
  float M = -INFINITY;
#pragma unroll
  for (int w = 0; w < WAVES; ++w) M = fmaxf(M, lds[(w * 2 + oh) * PREC]);
  float Ls = 0.f;
  f32x2 o = {0.f, 0.f};
#pragma unroll
  for (int w = 0; w < WAVES; ++w) {
    const float* rec = lds + (w * 2 + oh) * PREC;
    const float f = __builtin_amdgcn_exp2f(rec[0] - M);
    Ls = fmaf(f, rec[1], Ls);
    o += *reinterpret_cast<const f32x2*>(rec + 4 + od) * f;
  }
  const unsigned tag = (epoch + 2u) | 1u;
  STAMP(2 + 2 * NCH);
  if (!is_cons) {
    gu64* r = grec + slot * 2 * GREC;
    st_gran(r + oi, tag, o[0]);
    st_gran(r + 128 + oi, tag, o[1]);
    if (oi < 16) st_gran(r + 256 + oi, tag, (oi & 1) ? Ls : M);
  } else {
    for (int spin = 0; spin < (1 << 22); ++spin) {
      bool okp[3];
#pragma unroll
      for (int pr = 0; pr < 3; ++pr) {
        bool ok = true;
#pragma unroll
        for (int e = 0; e < 4; ++e) ok &= ((unsigned)(g[pr][e] >> 32) == tag);
        okp[pr] = __all(ok);
      }
      if (okp[0] && okp[1] && okp[2]) break;
      if (spin) __builtin_amdgcn_s_sleep(CFG_SLEEP);
#pragma unroll
      for (int pr = 0; pr < 3; ++pr) {
        if (CFG_SKIPVALID && okp[pr]) continue;
        gu64* r = grec + pr * 2 * GREC;
        g[pr][0] = ld_gran(r + 256); g[pr][1] = ld_gran(r + 257); g[pr][2] = ld_gran(r + oi); g[pr][3] = ld_gran(r + 128 + oi);
      }
    }
    if (tid == 0) __hip_atomic_store(ep, tag, __ATOMIC_RELAXED, __HIP_MEMORY_SCOPE_AGENT);
    float Mt = M;
#pragma unroll
    for (int pr = 0; pr < 3; ++pr) Mt = fmaxf(Mt, __uint_as_float((unsigned)g[pr][0]));
    const float f = __builtin_amdgcn_exp2f(M - Mt);
    float Lt = Ls * f;
    o *= f;
#pragma unroll
    for (int pr = 0; pr < 3; ++pr) {
      const float fp = __builtin_amdgcn_exp2f(__uint_as_float((unsigned)g[pr][0]) - Mt);
      Lt = fmaf(fp, __uint_as_float((unsigned)g[pr][1]), Lt);
      o += f32x2{__uint_as_float((unsigned)g[pr][2]), __uint_as_float((unsigned)g[pr][3])} * fp;
    }
    const float inv = 1.f / Lt;
    *reinterpret_cast<unsigned*>(out + ((size_t)pair * 2 + oh) * DH + od) = cvt_pk(o * inv);
  }
}

}

extern "C" void kernel_launch(void* const* d_in, const int* in_sizes, int n_in, void* d_out, int out_size, void* d_ws,
                              size_t ws_size, hipStream_t stream) {
  const unsigned short* q = (const unsigned short*)d_in[0];
  const int* qk = (const int*)d_in[1];
  const int* qv = (const int*)d_in[2];
  const unsigned short* ks = (const unsigned short*)d_in[3];
  const unsigned short* kz = (const unsigned short*)d_in[4];
  const unsigned short* vs = (const unsigned short*)d_in[5];
  const unsigned short* vz = (const unsigned short*)d_in[6];
  attn_fused<<<dim3(NPAIR * NSPLIT), dim3(WAVES * 64), 0, stream>>>(q, qk, qv, ks, kz, vs, vz, (unsigned char*)d_ws,
                                                                    (unsigned short*)d_out);
}
```

```cpp
#include <hip/hip_runtime.h>
#include <hip/hip_bf16.h>
#include <stdint.h>

#define CFG_CH 32
#define CFG_U 4
#define CFG_PAIRSTEP 2
#define CFG_LDSPAD (96 * 1024)
#define CFG_CONS 3
#define CFG_PREF 1
#define CFG_SLEEP 8
#define CFG_CLEAN 1
#define CFG_SKIPVALID 1
#define AUXK 2
#define AUXV 2

#define CFG_SMALLNT 0
#define LDS_(p) (*(p))

namespace {

constexpr int B = 8, HQ = 16, HKV = 8, SKV = 4096, DH = 256, G = 128, NG = SKV / G, REP = HQ / HKV;
constexpr int WAVES = 4, CH = CFG_CH, U = CFG_U, TPB = 1024;
constexpr int CT = CH * WAVES;
constexpr int NCH = TPB / CT;
constexpr int NSPLIT = SKV / TPB;
constexpr int NPAIR = B * HKV;
constexpr int PS = CFG_PAIRSTEP;
constexpr int CONS = CFG_CONS;
static_assert(CT <= G && G % CT == 0, "a chunk stays inside one K group");
static_assert(CH == 16 || CH == 32, "chunk");
static_assert(REP == 2 && NSPLIT == 4, "two query heads per kv head, four splits");

typedef int i32x4 __attribute__((ext_vector_type(4)));
typedef unsigned u32x2 __attribute__((ext_vector_type(2)));
typedef float f32x2 __attribute__((ext_vector_type(2)));
typedef float f32x4 __attribute__((ext_vector_type(4)));
typedef __bf16 bf16x2 __attribute__((ext_vector_type(2)));
typedef __amdgpu_buffer_rsrc_t rsrc_t;
typedef unsigned long long u64;
typedef __attribute__((address_space(1))) u64 gu64;
typedef __attribute__((address_space(1))) unsigned gu32;

__device__ __forceinline__ float bf_lo(unsigned u) { return __builtin_bit_cast(float, u << 16); }
__device__ __forceinline__ float bf_hi(unsigned u) { return __builtin_bit_cast(float, u & 0xffff0000u); }
__device__ __forceinline__ unsigned cvt_pk(f32x2 v) {
  bf16x2 r = __builtin_convertvector(v, bf16x2);
  return __builtin_bit_cast(unsigned, r);
}
__device__ __forceinline__ f32x2 unpk(unsigned u) { return f32x2{bf_lo(u), bf_hi(u)}; }
__device__ __forceinline__ float dot2(unsigned a, unsigned b, float c) {
  return __builtin_amdgcn_fdot2_f32_bf16(__builtin_bit_cast(bf16x2, a), __builtin_bit_cast(bf16x2, b), c, false);
}
template <int CTRL> __device__ __forceinline__ float dpp(float x) {
  return __builtin_bit_cast(float, __builtin_amdgcn_mov_dpp(__builtin_bit_cast(int, x), CTRL, 0xf, 0xf, true));
}
constexpr int XOR1 = 0xB1, XOR2 = 0x4E, XOR7 = 0x141, XOR8 = 0x128;

__device__ __forceinline__ float comb32(float a, float b) {
  auto s = __builtin_amdgcn_permlane32_swap(__float_as_uint(a), __float_as_uint(b), false, false);
  return __uint_as_float(s[0]) + __uint_as_float(s[1]);
}
__device__ __forceinline__ float comb16(float a, float b) {
  auto s = __builtin_amdgcn_permlane16_swap(__float_as_uint(a), __float_as_uint(b), false, false);
  return __uint_as_float(s[0]) + __uint_as_float(s[1]);
}
template <int CTRL> __device__ __forceinline__ float combdpp(float a, float b, bool hi) {
  const float keep = hi ? b : a, send = hi ? a : b;
  return keep + dpp<CTRL>(send);
}
__device__ __forceinline__ float half_max(float x) {
  x = fmaxf(x, dpp<XOR1>(x));
  x = fmaxf(x, dpp<XOR2>(x));
  x = fmaxf(x, dpp<XOR7>(x));
  x = fmaxf(x, dpp<XOR8>(x));
  auto s = __builtin_amdgcn_permlane16_swap(__float_as_uint(x), __float_as_uint(x), false, false);
  return fmaxf(__uint_as_float(s[0]), __uint_as_float(s[1]));
}
__device__ __forceinline__ float half_sum(float x) {
  x += dpp<XOR1>(x);
  x += dpp<XOR2>(x);
  x += dpp<XOR7>(x);
  x += dpp<XOR8>(x);
  auto s = __builtin_amdgcn_permlane16_swap(__float_as_uint(x), __float_as_uint(x), false, false);
  return __uint_as_float(s[0]) + __uint_as_float(s[1]);
}
__device__ __forceinline__ float rdlane(float x, int l) {
  return __builtin_bit_cast(float, __builtin_amdgcn_readlane(__builtin_bit_cast(int, x), l));
}
__host__ __device__ constexpr int roff(int j) { return ((j / U) * U * WAVES + (j % U)) * 1024; }
template <int AUX> __device__ __forceinline__ i32x4 ldrow(rsrc_t r, int voff, int sbase, int j) {
  return __builtin_amdgcn_raw_buffer_load_b128(r, voff + (roff(j) & 4095), sbase + (roff(j) & ~4095), AUX);
}
__host__ __device__ constexpr int brev5(int j) {
  return ((j & 1) << 4) | ((j & 2) << 2) | (j & 4) | ((j & 8) >> 2) | ((j & 16) >> 4);
}
__device__ __forceinline__ void st_gran(gu64* p, unsigned tag, float v) {
  __hip_atomic_store(p, ((u64)tag << 32) | (u64)__float_as_uint(v), __ATOMIC_RELAXED, __HIP_MEMORY_SCOPE_AGENT);
}
__device__ __forceinline__ u64 ld_gran(gu64* p) {
  return __hip_atomic_load(p, __ATOMIC_RELAXED, __HIP_MEMORY_SCOPE_AGENT);
}

constexpr int PREC = DH + 4;
constexpr int GREC = 272;
constexpr size_t WS_GRAN = 16384;

__global__ void __launch_bounds__(WAVES * 64)
attn_fused(const unsigned short* __restrict__ q, const int* __restrict__ qk, const int* __restrict__ qv,
           const unsigned short* __restrict__ ksc, const unsigned short* __restrict__ kzr,
           const unsigned short* __restrict__ vsc, const unsigned short* __restrict__ vzr,
           unsigned char* __restrict__ ws, unsigned short* __restrict__ out) {
  __shared__ __attribute__((aligned(16))) float lds[(CFG_LDSPAD > WAVES * 2 * PREC * 4 ? CFG_LDSPAD : WAVES * 2 * PREC * 4) / 4];
  const int tid = threadIdx.x, lane = tid & 63;
  const int wave = __builtin_amdgcn_readfirstlane(tid >> 6);
  const int bid = blockIdx.x;
#define STAMP(i)
  STAMP(0);
  const int pair = bid / NSPLIT, split = bid % NSPLIT;
  const size_t blockrow = (size_t)bid * TPB;
  const size_t waverow = blockrow + wave * U;
  const int nbytes = (TPB - wave * U) * 1024;
  const rsrc_t kp = __builtin_amdgcn_make_buffer_rsrc((void*)(qk + waverow * DH), 0, nbytes, 0x00020000);
  const rsrc_t vp = __builtin_amdgcn_make_buffer_rsrc((void*)(qv + waverow * DH), 0, nbytes, 0x00020000);
  const int voff = lane * 16;

  const u32x2 q0 = LDS_(reinterpret_cast<const u32x2*>(q + ((size_t)pair * 2 + 0) * DH + lane * 4));
  const u32x2 q1 = LDS_(reinterpret_cast<const u32x2*>(q + ((size_t)pair * 2 + 1) * DH + lane * 4));
  const size_t gbase = ((size_t)pair * NG + split * (TPB / G)) * DH;
  const unsigned short* ksb = ksc + gbase + lane * 4;
  const unsigned short* kzb = kzr + gbase + lane * 4;
  const int jl = (CH == 32) ? brev5(lane & 31) : (brev5(lane & 31) & 15);
  const int tl = (jl / U) * U * WAVES + (jl % U);
  const unsigned short* vsb = vsc + waverow + tl;
  const unsigned short* vzb = vzr + waverow + tl;
  u32x2 ksn = LDS_(reinterpret_cast<const u32x2*>(ksb));
  u32x2 kzn = LDS_(reinterpret_cast<const u32x2*>(kzb));
  unsigned short vsn = LDS_(vsb), vzn = LDS_(vzb);

  i32x4 buf[CH];
#pragma unroll
  for (int s = 0; s < CH; ++s) {
    __builtin_amdgcn_sched_barrier(0);
    buf[s] = ldrow<AUXK>(kp, voff, 0, s);
  }
  __builtin_amdgcn_sched_barrier(0);

  gu32* ep = (gu32*)(ws + (size_t)pair * 128);
  const unsigned epoch = __hip_atomic_load(ep, __ATOMIC_RELAXED, __HIP_MEMORY_SCOPE_AGENT);
  const int oh = tid >> 7, oi = tid & 127, od = oi * 2;
  gu64* grec = (gu64*)(ws + WS_GRAN) + ((size_t)pair * 3 * 2 + oh) * GREC;
  const bool is_cons = (split == CONS);
  const int slot = split < CONS ? split : split - 1;
  if (CFG_CLEAN && !is_cons) {
    gu64* r = grec + slot * 2 * GREC;
    st_gran(r + oi, 0u, 0.f);
    st_gran(r + 128 + oi, 0u, 0.f);
    if (oi < 16) st_gran(r + 256 + oi, 0u, 0.f);
  }
  __builtin_amdgcn_sched_barrier(0);

  float m = -INFINITY, l = 0.f, cc = 0.f;
  f32x2 a0l = {0.f, 0.f}, a0h = a0l, a1l = a0l, a1h = a0l;
  const bool b8 = lane & 8, b4 = lane & 4, b2 = lane & 2, b1 = lane & 1;
  u64 g[3][4];
#pragma unroll
  for (int p = 0; p < 3; ++p)
#pragma unroll
    for (int e = 0; e < 4; ++e) g[p][e] = 0;

#pragma unroll 1
  for (int c = 0; c < NCH; ++c) {
    const int sb = c * CT * 1024;
    const f32x2 ksl = unpk(ksn[0]), ksh = unpk(ksn[1]), kzl = unpk(kzn[0]), kzh = unpk(kzn[1]);
    const float vs = bf_lo(vsn), vz = bf_lo(vzn);
    {
      const int cn = (c + 1 < NCH) ? c + 1 : c;
      const int gn = (cn * CT) / G;
      ksn = LDS_(reinterpret_cast<const u32x2*>(ksb + gn * DH));
      kzn = LDS_(reinterpret_cast<const u32x2*>(kzb + gn * DH));
      vsn = LDS_(vsb + cn * CT);
      vzn = LDS_(vzb + cn * CT);
    }
    float lvl[5];
    float sc = 0.f;
#pragma unroll
    for (int s0 = 0; s0 < CH; s0 += PS) {
      __builtin_amdgcn_sched_barrier(0);
      f32x2 xl[PS], xh[PS];
#pragma unroll
      for (int e = 0; e < PS; ++e) {
        const i32x4 x = buf[s0 + e];
        xl[e] = f32x2{(float)x[0], (float)x[1]};
        xh[e] = f32x2{(float)x[2], (float)x[3]};
        asm volatile("" : "+v"(xl[e]), "+v"(xh[e]));
        buf[s0 + e] = ldrow<AUXV>(vp, voff, sb, s0 + e);
      }
      if (s0 == 0) STAMP(1 + 2 * c);
#pragma unroll
      for (int e = 0; e < PS; ++e) {
        const int s = s0 + e;
        const unsigned t01 = cvt_pk(xl[e] - kzl), t23 = cvt_pk(xh[e] - kzh);
        const unsigned k01 = cvt_pk(unpk(t01) * ksl), k23 = cvt_pk(unpk(t23) * ksh);
        const float d0 = dot2(k23, q0[1], dot2(k01, q0[0], 0.f));
        const float d1 = dot2(k23, q1[1], dot2(k01, q1[0], 0.f));
        float v = comb32(d0, d1);
        if ((s & 1) == 0) { lvl[0] = v; continue; }
        v = comb16(lvl[0], v);
        if ((s & 2) == 0) { lvl[1] = v; continue; }
        v = combdpp<XOR8>(lvl[1], v, b8);
        if ((s & 4) == 0) { lvl[2] = v; continue; }
        v = combdpp<XOR7>(lvl[2], v, b4);
        if ((s & 8) == 0) { lvl[3] = v; continue; }
        v = combdpp<XOR2>(lvl[3], v, b2);
        if (CH == 16) { sc = v + dpp<XOR1>(v); continue; }
        if ((s & 16) == 0) { lvl[4] = v; continue; }
        sc = combdpp<XOR1>(lvl[4], v, b1);
      }
    }
    __builtin_amdgcn_sched_barrier(0);
    sc = bf_lo(cvt_pk(f32x2{sc, sc}));
    constexpr float C2 = 0.0625f * 1.4426950408889634f;
    const float xs = sc * C2;
    const float mn = fmaxf(m, half_max(xs));
    const float alpha = __builtin_amdgcn_exp2f(m - mn);
    float p = __builtin_amdgcn_exp2f(xs - mn);
    m = mn;
    const float w = p * vs;
    l = fmaf(l, alpha, p);
    cc = fmaf(cc, alpha, w * vz);
    const float al0 = rdlane(alpha, 0), al1 = rdlane(alpha, 32);
    a0l *= al0; a0h *= al0; a1l *= al1; a1h *= al1;
    const int sbn = sb + CT * 1024;
    if (is_cons && c == NCH - 1) {
#pragma unroll
      for (int pr = 0; pr < 3; ++pr) {
        gu64* r = grec + pr * 2 * GREC;
        g[pr][0] = ld_gran(r + 256); g[pr][1] = ld_gran(r + 257); g[pr][2] = ld_gran(r + oi); g[pr][3] = ld_gran(r + 128 + oi);
      }
    }
#pragma unroll
    for (int j0 = 0; j0 < CH; j0 += PS) {
      __builtin_amdgcn_sched_barrier(0);
      f32x2 xl[PS], xh[PS];
#pragma unroll
      for (int e = 0; e < PS; ++e) {
        const i32x4 x = buf[j0 + e];
        xl[e] = f32x2{(float)x[0], (float)x[1]};
        xh[e] = f32x2{(float)x[2], (float)x[3]};
        asm volatile("" : "+v"(xl[e]), "+v"(xh[e]));
        buf[j0 + e] = ldrow<AUXK>(kp, voff, sbn, j0 + e);
      }
      if (j0 == 0) STAMP(2 + 2 * c);
#pragma unroll
      for (int e = 0; e < PS; ++e) {
        const int j = j0 + e;
        const float w0 = rdlane(w, brev5(j)), w1 = rdlane(w, 32 + brev5(j));
        const f32x2 w0v = {w0, w0}, w1v = {w1, w1};
        a0l = __builtin_elementwise_fma(w0v, xl[e], a0l);
        a0h = __builtin_elementwise_fma(w0v, xh[e], a0h);
        a1l = __builtin_elementwise_fma(w1v, xl[e], a1l);
        a1h = __builtin_elementwise_fma(w1v, xh[e], a1h);
      }
      asm volatile("" : "+v"(a0l), "+v"(a0h), "+v"(a1l), "+v"(a1h));
    }
  }
  STAMP(1 + 2 * NCH);
  float L = half_sum(l), C = half_sum(cc);
  if (CH == 16) { L *= 0.5f; C *= 0.5f; }
  const float c0 = rdlane(C, 0), c1 = rdlane(C, 32);
  float* rec0 = lds + (wave * 2 + 0) * PREC;
  float* rec1 = lds + (wave * 2 + 1) * PREC;
  *reinterpret_cast<f32x4*>(rec0 + 4 + lane * 4) = f32x4{a0l[0] - c0, a0l[1] - c0, a0h[0] - c0, a0h[1] - c0};
  *reinterpret_cast<f32x4*>(rec1 + 4 + lane * 4) = f32x4{a1l[0] - c1, a1l[1] - c1, a1h[0] - c1, a1h[1] - c1};
  if ((lane & 31) == 0) {
    float* rec = lds + (wave * 2 + (lane >> 5)) * PREC;
    rec[0] = m; rec[1] = L;
  }
  __syncthreads();
  float M = -INFINITY;
#pragma unroll
  for (int w = 0; w < WAVES; ++w) M = fmaxf(M, lds[(w * 2 + oh) * PREC]);
  float Ls = 0.f;
  f32x2 o = {0.f, 0.f};
#pragma unroll
  for (int w = 0; w < WAVES; ++w) {
    const float* rec = lds + (w * 2 + oh) * PREC;
    const float f = __builtin_amdgcn_exp2f(rec[0] - M);
    Ls = fmaf(f, rec[1], Ls);
    o += *reinterpret_cast<const f32x2*>(rec + 4 + od) * f;
  }
  const unsigned tag = (epoch + 2u) | 1u;
  STAMP(2 + 2 * NCH);
  if (!is_cons) {
    gu64* r = grec + slot * 2 * GREC;
    st_gran(r + oi, tag, o[0]);
    st_gran(r + 128 + oi, tag, o[1]);
    if (oi < 16) st_gran(r + 256 + oi, tag, (oi & 1) ? Ls : M);
  } else {
    for (int spin = 0; spin < (1 << 22); ++spin) {
      bool okp[3];
#pragma unroll
      for (int pr = 0; pr < 3; ++pr) {
        bool ok = true;
#pragma unroll
        for (int e = 0; e < 4; ++e) ok &= ((unsigned)(g[pr][e] >> 32) == tag);
        okp[pr] = __all(ok);
      }
      if (okp[0] && okp[1] && okp[2]) break;
      if (spin) __builtin_amdgcn_s_sleep(CFG_SLEEP);
#pragma unroll
      for (int pr = 0; pr < 3; ++pr) {
        if (CFG_SKIPVALID && okp[pr]) continue;
        gu64* r = grec + pr * 2 * GREC;
        g[pr][0] = ld_gran(r + 256); g[pr][1] = ld_gran(r + 257); g[pr][2] = ld_gran(r + oi); g[pr][3] = ld_gran(r + 128 + oi);
      }
    }
    if (tid == 0) __hip_atomic_store(ep, tag, __ATOMIC_RELAXED, __HIP_MEMORY_SCOPE_AGENT);
    float Mt = M;
#pragma unroll
    for (int pr = 0; pr < 3; ++pr) Mt = fmaxf(Mt, __uint_as_float((unsigned)g[pr][0]));
    const float f = __builtin_amdgcn_exp2f(M - Mt);
    float Lt = Ls * f;
    o *= f;
#pragma unroll
    for (int pr = 0; pr < 3; ++pr) {
      const float fp = __builtin_amdgcn_exp2f(__uint_as_float((unsigned)g[pr][0]) - Mt);
      Lt = fmaf(fp, __uint_as_float((unsigned)g[pr][1]), Lt);
      o += f32x2{__uint_as_float((unsigned)g[pr][2]), __uint_as_float((unsigned)g[pr][3])} * fp;
    }
    const float inv = 1.f / Lt;
    *reinterpret_cast<unsigned*>(out + ((size_t)pair * 2 + oh) * DH + od) = cvt_pk(o * inv);
  }
}

}

extern "C" void kernel_launch(void* const* d_in, const int* in_sizes, int n_in, void* d_out, int out_size, void* d_ws,
                              size_t ws_size, hipStream_t stream) {
  const unsigned short* q = (const unsigned short*)d_in[0];
  const int* qk = (const int*)d_in[1];
  const int* qv = (const int*)d_in[2];
  const unsigned short* ks = (const unsigned short*)d_in[3];
  const unsigned short* kz = (const unsigned short*)d_in[4];
  const unsigned short* vs = (const unsigned short*)d_in[5];
  const unsigned short* vz = (const unsigned short*)d_in[6];
  attn_fused<<<dim3(NPAIR * NSPLIT), dim3(WAVES * 64), 0, stream>>>(q, qk, qv, ks, kz, vs, vz, (unsigned char*)d_ws,
                                                                    (unsigned short*)d_out);
}
```

```cpp
#include <hip/hip_runtime.h>
#include <hip/hip_bf16.h>
#include <stdint.h>

#define CFG_W 16
#define CFG_U 4
#define CFG_PS 2
#define CFG_LDSPAD (96 * 1024)
#define CFG_FUSED 1
#define CFG_CONS 3
#define CFG_PREF 0
#define CFG_SLEEP 4
#define CFG_CLEAN 1
#define CFG_SMALLNT 1
#define LDSM(p) __builtin_nontemporal_load(p)

namespace {

constexpr int B = 8, HQ = 16, HKV = 8, SKV = 4096, DH = 256, G = 128, NG = SKV / G, REP = HQ / HKV;
constexpr int WAVES = 4, CH = 32, W = CFG_W, U = CFG_U, TPB = 1024;
constexpr int CT = CH * WAVES;
constexpr int NCH = TPB / CT;
constexpr int NSPLIT = SKV / TPB;
constexpr int NPAIR = B * HKV;
constexpr int PS = CFG_PS;
constexpr int CONS = CFG_CONS;
static_assert(CT == G, "a chunk is one K group");
static_assert(CH % W == 0 && W % U == 0 && W % PS == 0, "ring");
static_assert(REP == 2 && NSPLIT == 4, "two query heads per kv head, four splits");

typedef int i32x4 __attribute__((ext_vector_type(4)));
typedef unsigned u32x2 __attribute__((ext_vector_type(2)));
typedef float f32x2 __attribute__((ext_vector_type(2)));
typedef float f32x4 __attribute__((ext_vector_type(4)));
typedef __bf16 bf16x2 __attribute__((ext_vector_type(2)));
typedef __amdgpu_buffer_rsrc_t rsrc_t;
typedef unsigned long long u64;
typedef __attribute__((address_space(1))) u64 gu64;
typedef __attribute__((address_space(1))) unsigned gu32;

__device__ __forceinline__ float bf_lo(unsigned u) { return __builtin_bit_cast(float, u << 16); }
__device__ __forceinline__ float bf_hi(unsigned u) { return __builtin_bit_cast(float, u & 0xffff0000u); }
__device__ __forceinline__ unsigned cvt_pk(f32x2 v) {
  bf16x2 r = __builtin_convertvector(v, bf16x2);
  return __builtin_bit_cast(unsigned, r);
}
__device__ __forceinline__ f32x2 unpk(unsigned u) { return f32x2{bf_lo(u), bf_hi(u)}; }
__device__ __forceinline__ float dot2(unsigned a, unsigned b, float c) {
  return __builtin_amdgcn_fdot2_f32_bf16(__builtin_bit_cast(bf16x2, a), __builtin_bit_cast(bf16x2, b), c, false);
}
template <int CTRL> __device__ __forceinline__ float dpp(float x) {
  return __builtin_bit_cast(float, __builtin_amdgcn_mov_dpp(__builtin_bit_cast(int, x), CTRL, 0xf, 0xf, true));
}
constexpr int XOR1 = 0xB1, XOR2 = 0x4E, XOR7 = 0x141, XOR8 = 0x128;

__device__ __forceinline__ float comb32(float a, float b) {
  auto s = __builtin_amdgcn_permlane32_swap(__float_as_uint(a), __float_as_uint(b), false, false);
  return __uint_as_float(s[0]) + __uint_as_float(s[1]);
}
__device__ __forceinline__ float comb16(float a, float b) {
  auto s = __builtin_amdgcn_permlane16_swap(__float_as_uint(a), __float_as_uint(b), false, false);
  return __uint_as_float(s[0]) + __uint_as_float(s[1]);
}
template <int CTRL> __device__ __forceinline__ float combdpp(float a, float b, bool hi) {
  const float keep = hi ? b : a, send = hi ? a : b;
  return keep + dpp<CTRL>(send);
}
__device__ __forceinline__ float half_max(float x) {
  x = fmaxf(x, dpp<XOR1>(x));
  x = fmaxf(x, dpp<XOR2>(x));
  x = fmaxf(x, dpp<XOR7>(x));
  x = fmaxf(x, dpp<XOR8>(x));
  auto s = __builtin_amdgcn_permlane16_swap(__float_as_uint(x), __float_as_uint(x), false, false);
  return fmaxf(__uint_as_float(s[0]), __uint_as_float(s[1]));
}
__device__ __forceinline__ float half_sum(float x) {
  x += dpp<XOR1>(x);
  x += dpp<XOR2>(x);
  x += dpp<XOR7>(x);
  x += dpp<XOR8>(x);
  auto s = __builtin_amdgcn_permlane16_swap(__float_as_uint(x), __float_as_uint(x), false, false);
  return __uint_as_float(s[0]) + __uint_as_float(s[1]);
}
__device__ __forceinline__ float rdlane(float x, int l) {
  return __builtin_bit_cast(float, __builtin_amdgcn_readlane(__builtin_bit_cast(int, x), l));
}
__host__ __device__ constexpr int roff(int j) { return ((j / U) * U * WAVES + (j % U)) * 1024; }
__device__ __forceinline__ i32x4 ldrow(rsrc_t r, int voff, int sbase, int j) {
  return __builtin_amdgcn_raw_buffer_load_b128(r, voff + (roff(j) & 4095), sbase + (roff(j) & ~4095), 2  );
}
__host__ __device__ constexpr int brev5(int j) {
  return ((j & 1) << 4) | ((j & 2) << 2) | (j & 4) | ((j & 8) >> 2) | ((j & 16) >> 4);
}
__device__ __forceinline__ void st_gran(gu64* p, unsigned tag, float v) {
  __hip_atomic_store(p, ((u64)tag << 32) | (u64)__float_as_uint(v), __ATOMIC_RELAXED, __HIP_MEMORY_SCOPE_AGENT);
}
__device__ __forceinline__ u64 ld_gran(gu64* p) {
  return __hip_atomic_load(p, __ATOMIC_RELAXED, __HIP_MEMORY_SCOPE_AGENT);
}

constexpr int PREC = DH + 4;
constexpr int GREC = 272;
constexpr size_t WS_GRAN = 16384;

__global__ void __launch_bounds__(WAVES * 64)
attn_main(const unsigned short* __restrict__ q, const int* __restrict__ qk, const int* __restrict__ qv,
          const unsigned short* __restrict__ ksc, const unsigned short* __restrict__ kzr,
          const unsigned short* __restrict__ vsc, const unsigned short* __restrict__ vzr,
          unsigned char* __restrict__ ws, unsigned short* __restrict__ out) {
  __shared__ __attribute__((aligned(16))) float lds[(CFG_LDSPAD > WAVES * 2 * PREC * 4 ? CFG_LDSPAD : WAVES * 2 * PREC * 4) / 4];
  const int tid = threadIdx.x, lane = tid & 63;
  const int wave = __builtin_amdgcn_readfirstlane(tid >> 6);
  const int bid = blockIdx.x;
#define STAMP(i)
  STAMP(0);
  const int pair = bid / NSPLIT, split = bid % NSPLIT;
  const size_t blockrow = (size_t)bid * TPB;
  const size_t waverow = blockrow + wave * U;
  const int nbytes = (TPB - wave * U) * 1024;
  const rsrc_t kp = __builtin_amdgcn_make_buffer_rsrc((void*)(qk + waverow * DH), 0, nbytes, 0x00020000);
  const rsrc_t vp = __builtin_amdgcn_make_buffer_rsrc((void*)(qv + waverow * DH), 0, nbytes, 0x00020000);
  const int voff = lane * 16;

  const u32x2 q0 = LDSM(reinterpret_cast<const u32x2*>(q + ((size_t)pair * 2 + 0) * DH + lane * 4));
  const u32x2 q1 = LDSM(reinterpret_cast<const u32x2*>(q + ((size_t)pair * 2 + 1) * DH + lane * 4));
  const size_t gbase = ((size_t)pair * NG + split * NCH) * DH;
  const unsigned short* ksb = ksc + gbase + lane * 4;
  const unsigned short* kzb = kzr + gbase + lane * 4;
  const int jl = brev5(lane & 31);
  const int tl = (jl / U) * U * WAVES + (jl % U);
  const unsigned short* vsb = vsc + waverow + tl;
  const unsigned short* vzb = vzr + waverow + tl;
  u32x2 ksn = LDSM(reinterpret_cast<const u32x2*>(ksb));
  u32x2 kzn = LDSM(reinterpret_cast<const u32x2*>(kzb));

  i32x4 buf[W];
#pragma unroll
  for (int s = 0; s < W; ++s) {
    __builtin_amdgcn_sched_barrier(0);
    buf[s] = ldrow(kp, voff, 0, s);
  }
  __builtin_amdgcn_sched_barrier(0);

  const int oh = tid >> 7, oi = tid & 127, od = oi * 2;
  gu32* ep = (gu32*)(ws + (size_t)pair * 128);
  const unsigned epoch = __hip_atomic_load(ep, __ATOMIC_RELAXED, __HIP_MEMORY_SCOPE_AGENT);
  gu64* grec = (gu64*)(ws + WS_GRAN) + ((size_t)pair * 3 * 2 + oh) * GREC;
  const bool is_cons = (split == CONS);
  const int slot = split < CONS ? split : split - 1;
  if (CFG_CLEAN && !is_cons) {
    gu64* r = grec + slot * 2 * GREC;
    st_gran(r + oi, 0u, 0.f);
    st_gran(r + 128 + oi, 0u, 0.f);
    if (oi < 16) st_gran(r + 256 + oi, 0u, 0.f);
  }
  u64 g[3][4];
#pragma unroll
  for (int p = 0; p < 3; ++p)
#pragma unroll
    for (int e = 0; e < 4; ++e) g[p][e] = 0;
  __builtin_amdgcn_sched_barrier(0);

  const bool b8 = lane & 8, b4 = lane & 4, b2 = lane & 2, b1 = lane & 1;
  float sr[NCH];
  unsigned zr[NCH];
#pragma unroll
  for (int i = 0; i < NCH; ++i) { sr[i] = 0.f; zr[i] = 0u; }

#pragma unroll 1
  for (int c = 0; c < NCH; ++c) {
    const int sb = c * CT * 1024;
    const bool lastc = (c + 1 == NCH);
    const rsrc_t np = lastc ? vp : kp;
    const int sbn = lastc ? 0 : sb + CT * 1024;
    const f32x2 ksl = unpk(ksn[0]), ksh = unpk(ksn[1]), kzl = unpk(kzn[0]), kzh = unpk(kzn[1]);
    const unsigned short vsn = LDSM(vsb + c * CT), vzn = LDSM(vzb + c * CT);
    {
      const int cn = lastc ? c : c + 1;
      ksn = LDSM(reinterpret_cast<const u32x2*>(ksb + cn * DH));
      kzn = LDSM(reinterpret_cast<const u32x2*>(kzb + cn * DH));
    }
    float lvl[5];
    float sc = 0.f;
#pragma unroll
    for (int s0 = 0; s0 < CH; s0 += PS) {
      __builtin_amdgcn_sched_barrier(0);
      f32x2 xl[PS], xh[PS];
#pragma unroll
      for (int e = 0; e < PS; ++e) {
        const int s = s0 + e;
        const i32x4 x = buf[s % W];
        xl[e] = f32x2{(float)x[0], (float)x[1]};
        xh[e] = f32x2{(float)x[2], (float)x[3]};
        asm volatile("" : "+v"(xl[e]), "+v"(xh[e]));
        if (s + W < CH) buf[s % W] = ldrow(kp, voff, sb, s + W);
        else buf[s % W] = ldrow(np, voff, sbn, s + W - CH);
      }
      if (s0 == 0) STAMP(1 + c);
#pragma unroll
      for (int e = 0; e < PS; ++e) {
        const int s = s0 + e;
        const unsigned t01 = cvt_pk(xl[e] - kzl), t23 = cvt_pk(xh[e] - kzh);
        const unsigned k01 = cvt_pk(unpk(t01) * ksl), k23 = cvt_pk(unpk(t23) * ksh);
        const float d0 = dot2(k23, q0[1], dot2(k01, q0[0], 0.f));
        const float d1 = dot2(k23, q1[1], dot2(k01, q1[0], 0.f));
        float v = comb32(d0, d1);
        if ((s & 1) == 0) { lvl[0] = v; continue; }
        v = comb16(lvl[0], v);
        if ((s & 2) == 0) { lvl[1] = v; continue; }
        v = combdpp<XOR8>(lvl[1], v, b8);
        if ((s & 4) == 0) { lvl[2] = v; continue; }
        v = combdpp<XOR7>(lvl[2], v, b4);
        if ((s & 8) == 0) { lvl[3] = v; continue; }
        v = combdpp<XOR2>(lvl[3], v, b2);
        if ((s & 16) == 0) { lvl[4] = v; continue; }
        sc = combdpp<XOR1>(lvl[4], v, b1);
      }
    }
    __builtin_amdgcn_sched_barrier(0);
#pragma unroll
    for (int i = 0; i + 1 < NCH; ++i) { sr[i] = sr[i + 1]; zr[i] = zr[i + 1]; }
    sr[NCH - 1] = sc;
    zr[NCH - 1] = (unsigned)vsn | ((unsigned)vzn << 16);
  }
  constexpr float C2 = 0.0625f * 1.4426950408889634f;
  float m = -INFINITY;
#pragma unroll
  for (int i = 0; i < NCH; i += 2) {
    const unsigned r = cvt_pk(f32x2{sr[i], sr[i + 1]});
    sr[i] = bf_lo(r) * C2; sr[i + 1] = bf_hi(r) * C2;
    m = fmaxf(m, fmaxf(sr[i], sr[i + 1]));
  }
  m = half_max(m);
  float l = 0.f, cc = 0.f;
#pragma unroll
  for (int i = 0; i < NCH; ++i) {
    const float p = __builtin_amdgcn_exp2f(sr[i] - m);
    l += p;
    sr[i] = p * bf_lo(zr[i]);
    cc = fmaf(sr[i], bf_hi(zr[i]), cc);
  }
  f32x2 a0l = {0.f, 0.f}, a0h = a0l, a1l = a0l, a1h = a0l;
#pragma unroll 1
  for (int c = 0; c < NCH; ++c) {
    const int sb = c * CT * 1024;
    const int sbn = sb + CT * 1024;
    const float w = sr[0];
#pragma unroll
    for (int j0 = 0; j0 < CH; j0 += PS) {
      __builtin_amdgcn_sched_barrier(0);
      f32x2 xl[PS], xh[PS];
#pragma unroll
      for (int e = 0; e < PS; ++e) {
        const int j = j0 + e;
        const i32x4 x = buf[j % W];
        xl[e] = f32x2{(float)x[0], (float)x[1]};
        xh[e] = f32x2{(float)x[2], (float)x[3]};
        asm volatile("" : "+v"(xl[e]), "+v"(xh[e]));
        if (j + W < CH) buf[j % W] = ldrow(vp, voff, sb, j + W);
        else buf[j % W] = ldrow(vp, voff, sbn, j + W - CH);
      }
      if (j0 == 0) STAMP(1 + NCH + c);
#pragma unroll
      for (int e = 0; e < PS; ++e) {
        const int j = j0 + e;
        const float w0 = rdlane(w, brev5(j)), w1 = rdlane(w, 32 + brev5(j));
        const f32x2 w0v = {w0, w0}, w1v = {w1, w1};
        a0l = __builtin_elementwise_fma(w0v, xl[e], a0l);
        a0h = __builtin_elementwise_fma(w0v, xh[e], a0h);
        a1l = __builtin_elementwise_fma(w1v, xl[e], a1l);
        a1h = __builtin_elementwise_fma(w1v, xh[e], a1h);
      }
      asm volatile("" : "+v"(a0l), "+v"(a0h), "+v"(a1l), "+v"(a1h));
    }
#pragma unroll
    for (int i = 0; i + 1 < NCH; ++i) sr[i] = sr[i + 1];
  }
  STAMP(1 + 2 * NCH);
  const float L = half_sum(l), C = half_sum(cc);
  const float c0 = rdlane(C, 0), c1 = rdlane(C, 32);
  float* rec0 = lds + (wave * 2 + 0) * PREC;
  float* rec1 = lds + (wave * 2 + 1) * PREC;
  *reinterpret_cast<f32x4*>(rec0 + 4 + lane * 4) = f32x4{a0l[0] - c0, a0l[1] - c0, a0h[0] - c0, a0h[1] - c0};
  *reinterpret_cast<f32x4*>(rec1 + 4 + lane * 4) = f32x4{a1l[0] - c1, a1l[1] - c1, a1h[0] - c1, a1h[1] - c1};
  if ((lane & 31) == 0) {
    float* rec = lds + (wave * 2 + (lane >> 5)) * PREC;
    rec[0] = m; rec[1] = L;
  }
  __syncthreads();
  float M = -INFINITY;
#pragma unroll
  for (int w = 0; w < WAVES; ++w) M = fmaxf(M, lds[(w * 2 + oh) * PREC]);
  float Ls = 0.f;
  f32x2 o = {0.f, 0.f};
#pragma unroll
  for (int w = 0; w < WAVES; ++w) {
    const float* rec = lds + (w * 2 + oh) * PREC;
    const float f = __builtin_amdgcn_exp2f(rec[0] - M);
    Ls = fmaf(f, rec[1], Ls);
    o += *reinterpret_cast<const f32x2*>(rec + 4 + od) * f;
  }
  STAMP(2 + 2 * NCH);
  const unsigned tag = (epoch + 2u) | 1u;
  if (!is_cons) {
    gu64* r = grec + slot * 2 * GREC;
    st_gran(r + oi, tag, o[0]);
    st_gran(r + 128 + oi, tag, o[1]);
    if (oi < 16) st_gran(r + 256 + oi, tag, (oi & 1) ? Ls : M);
  } else {
    for (int spin = 0; spin < (1 << 22); ++spin) {
      bool okp[3];
#pragma unroll
      for (int pr = 0; pr < 3; ++pr) {
        bool ok = true;
#pragma unroll
        for (int e = 0; e < 4; ++e) ok &= ((unsigned)(g[pr][e] >> 32) == tag);
        okp[pr] = __all(ok);
      }
      if (okp[0] && okp[1] && okp[2]) break;
      if (spin) __builtin_amdgcn_s_sleep(CFG_SLEEP);
#pragma unroll
      for (int pr = 0; pr < 3; ++pr) {
        if (okp[pr]) continue;
        gu64* r = grec + pr * 2 * GREC;
        g[pr][0] = ld_gran(r + 256); g[pr][1] = ld_gran(r + 257); g[pr][2] = ld_gran(r + oi); g[pr][3] = ld_gran(r + 128 + oi);
      }
    }
    if (tid == 0) __hip_atomic_store(ep, tag, __ATOMIC_RELAXED, __HIP_MEMORY_SCOPE_AGENT);
    float Mt = M;
#pragma unroll
    for (int pr = 0; pr < 3; ++pr) Mt = fmaxf(Mt, __uint_as_float((unsigned)g[pr][0]));
    const float f = __builtin_amdgcn_exp2f(M - Mt);
    float Lt = Ls * f;
    o *= f;
#pragma unroll
    for (int pr = 0; pr < 3; ++pr) {
      const float fp = __builtin_amdgcn_exp2f(__uint_as_float((unsigned)g[pr][0]) - Mt);
      Lt = fmaf(fp, __uint_as_float((unsigned)g[pr][1]), Lt);
      o += f32x2{__uint_as_float((unsigned)g[pr][2]), __uint_as_float((unsigned)g[pr][3])} * fp;
    }
    const float inv = 1.f / Lt;
    *reinterpret_cast<unsigned*>(out + ((size_t)pair * 2 + oh) * DH + od) = cvt_pk(o * inv);
  }
}

}

extern "C" void kernel_launch(void* const* d_in, const int* in_sizes, int n_in, void* d_out, int out_size, void* d_ws,
                              size_t ws_size, hipStream_t stream) {
  const unsigned short* q = (const unsigned short*)d_in[0];
  const int* qk = (const int*)d_in[1];
  const int* qv = (const int*)d_in[2];
  const unsigned short* ks = (const unsigned short*)d_in[3];
  const unsigned short* kz = (const unsigned short*)d_in[4];
  const unsigned short* vs = (const unsigned short*)d_in[5];
  const unsigned short* vz = (const unsigned short*)d_in[6];
  attn_main<<<dim3(NPAIR * NSPLIT), dim3(WAVES * 64), 0, stream>>>(q, qk, qv, ks, kz, vs, vz, (unsigned char*)d_ws,
                                                                   (unsigned short*)d_out);
}
```

```cpp
#include <hip/hip_runtime.h>
#include <hip/hip_bf16.h>
#include <stdint.h>

namespace {

constexpr int B = 8, HQ = 16, HKV = 8, SKV = 4096, DH = 256, G = 128, NG = SKV / G, REP = HQ / HKV;
constexpr int WAVES = 4;
constexpr int CH = 32;
constexpr int W = 8;
constexpr int U = 4;
constexpr int TPB = 1024;
constexpr int CT = CH * WAVES;
constexpr int NCH = TPB / CT;
constexpr int NSPLIT = SKV / TPB;
constexpr int NPAIR = B * HKV;
constexpr int D = 5;
constexpr int BOOST = 2;
constexpr int TAU = 438;
constexpr int JUMP = 120;
constexpr int CONS = 0;
constexpr int POLLC = 6;
constexpr int SPIN_CAP = 1 << 20;
constexpr int LDS_BYTES = 96 * 1024;
static_assert(CT == G, "a chunk is one K group");
static_assert(D < W && CH % W == 0 && W % U == 0, "ring");
static_assert(REP == 2 && NSPLIT == 4 && NCH == 8, "two query heads per kv head, four splits");

typedef int i32x4 __attribute__((ext_vector_type(4)));
typedef unsigned u32x2 __attribute__((ext_vector_type(2)));
typedef unsigned u32x4 __attribute__((ext_vector_type(4)));
typedef float f32x2 __attribute__((ext_vector_type(2)));
typedef float f32x4 __attribute__((ext_vector_type(4)));
typedef __bf16 bf16x2 __attribute__((ext_vector_type(2)));
typedef __amdgpu_buffer_rsrc_t rsrc_t;
typedef unsigned long long u64;
typedef __attribute__((address_space(1))) u64 gu64;
typedef __attribute__((address_space(1))) unsigned gu32;
typedef __attribute__((address_space(3))) unsigned lu32;

__device__ __forceinline__ float bf_lo(unsigned u) { return __builtin_bit_cast(float, u << 16); }
__device__ __forceinline__ float bf_hi(unsigned u) { return __builtin_bit_cast(float, u & 0xffff0000u); }
__device__ __forceinline__ unsigned cvt_pk(f32x2 v) {
  bf16x2 r = __builtin_convertvector(v, bf16x2);
  return __builtin_bit_cast(unsigned, r);
}
__device__ __forceinline__ f32x2 unpk(unsigned u) { return f32x2{bf_lo(u), bf_hi(u)}; }
__device__ __forceinline__ float dot2(unsigned a, unsigned b, float c) {
  return __builtin_amdgcn_fdot2_f32_bf16(__builtin_bit_cast(bf16x2, a), __builtin_bit_cast(bf16x2, b), c, false);
}
template <int CTRL> __device__ __forceinline__ float dpp(float x) {
  return __builtin_bit_cast(float, __builtin_amdgcn_mov_dpp(__builtin_bit_cast(int, x), CTRL, 0xf, 0xf, true));
}
constexpr int XOR1 = 0xB1, XOR2 = 0x4E, XOR7 = 0x141, XOR8 = 0x128;

__device__ __forceinline__ float comb32(float a, float b) {
  auto s = __builtin_amdgcn_permlane32_swap(__float_as_uint(a), __float_as_uint(b), false, false);
  return __uint_as_float(s[0]) + __uint_as_float(s[1]);
}
__device__ __forceinline__ float comb16(float a, float b) {
  auto s = __builtin_amdgcn_permlane16_swap(__float_as_uint(a), __float_as_uint(b), false, false);
  return __uint_as_float(s[0]) + __uint_as_float(s[1]);
}
template <int CTRL> __device__ __forceinline__ float combdpp(float a, float b, bool hi) {
  const float keep = hi ? b : a, send = hi ? a : b;
  return keep + dpp<CTRL>(send);
}
__device__ __forceinline__ float half_max(float x) {
  x = fmaxf(x, dpp<XOR1>(x));
  x = fmaxf(x, dpp<XOR2>(x));
  x = fmaxf(x, dpp<XOR7>(x));
  x = fmaxf(x, dpp<XOR8>(x));
  auto s = __builtin_amdgcn_permlane16_swap(__float_as_uint(x), __float_as_uint(x), false, false);
  return fmaxf(__uint_as_float(s[0]), __uint_as_float(s[1]));
}
__device__ __forceinline__ float half_sum(float x) {
  x += dpp<XOR1>(x);
  x += dpp<XOR2>(x);
  x += dpp<XOR7>(x);
  x += dpp<XOR8>(x);
  auto s = __builtin_amdgcn_permlane16_swap(__float_as_uint(x), __float_as_uint(x), false, false);
  return __uint_as_float(s[0]) + __uint_as_float(s[1]);
}
__device__ __forceinline__ float rdlane(float x, int l) {
  return __builtin_bit_cast(float, __builtin_amdgcn_readlane(__builtin_bit_cast(int, x), l));
}
__host__ __device__ constexpr int roff(int j) { return ((j / U) * U * WAVES + (j % U)) * 1024; }
__device__ __forceinline__ i32x4 ldrow(rsrc_t r, int voff, int sbase, int j) {
  return __builtin_amdgcn_raw_buffer_load_b128(r, voff + (roff(j) & 4095), sbase + (roff(j) & ~4095), 2  );
}
__host__ __device__ constexpr int brev5(int j) {
  return ((j & 1) << 4) | ((j & 2) << 2) | (j & 4) | ((j & 8) >> 2) | ((j & 16) >> 4);
}
__device__ __forceinline__ void gate(int s, int lag) {
  const int ph = (s & 3) == 0 ? 0 : (s & 3) == 2 ? 1 : (s & 3) == 1 ? 2 : 3;
  if (ph < BOOST) {
    asm volatile("s_cmp_gt_i32 %0, 0\n\ts_cbranch_scc1 1f\n\ts_waitcnt vmcnt(%1)\n1:\n\ts_waitcnt vmcnt(%2)"
                 :: "s"(lag), "n"(D - 1), "n"(D) : "memory", "scc");
  } else {
    asm volatile("s_waitcnt vmcnt(%0)" :: "n"(D - 1) : "memory");
  }
}
__device__ __forceinline__ void st_gran(gu64* p, unsigned tag, float v) {
  __hip_atomic_store(p, ((u64)tag << 32) | (u64)__float_as_uint(v), __ATOMIC_RELAXED, __HIP_MEMORY_SCOPE_AGENT);
}
__device__ __forceinline__ u64 ld_gran(gu64* p) {
  return __hip_atomic_load(p, __ATOMIC_RELAXED, __HIP_MEMORY_SCOPE_AGENT);
}
__device__ __forceinline__ float gval(u64 x) { return __uint_as_float((unsigned)x); }
__device__ __forceinline__ bool gok(u64 x, unsigned tag) { return (unsigned)(x >> 32) == tag; }
__device__ __forceinline__ unsigned ld_lds(lu32* p) {
  return __hip_atomic_load(p, __ATOMIC_RELAXED, __HIP_MEMORY_SCOPE_WORKGROUP);
}
__device__ __forceinline__ void st_lds(lu32* p, unsigned v) {
  __hip_atomic_store(p, v, __ATOMIC_RELAXED, __HIP_MEMORY_SCOPE_WORKGROUP);
}
__device__ __forceinline__ void lds_release() { __builtin_amdgcn_fence(__ATOMIC_RELEASE, "workgroup", "local"); }
__device__ __forceinline__ void lds_acquire() { __builtin_amdgcn_fence(__ATOMIC_ACQUIRE, "workgroup", "local"); }

constexpr int L_KS = 7 * 2 * DH, L_KZ = L_KS + 1024, L_VS = L_KZ + 1024, L_VZ = L_VS + 512, L_W = L_VZ + 512;
constexpr int W_TAG = 0, W_PROG = 1, W_MISS = 2, W_RDY = 3, W_FLAG = 4, W_CNT = 8, W_M = 16, W_L = 24, W_FW = 32,
              W_FR = 40, L_END = L_W + 48;
static_assert(L_END * 4 <= LDS_BYTES, "LDS");
constexpr int GREC = 528;
constexpr size_t WS_GRAN = 16384;

__global__ void __launch_bounds__((WAVES + 1) * 64)
kivi_decode_attn(const unsigned short* __restrict__ q, const int* __restrict__ qk, const int* __restrict__ qv,
                 const unsigned short* __restrict__ ksc, const unsigned short* __restrict__ kzr,
                 const unsigned short* __restrict__ vsc, const unsigned short* __restrict__ vzr,
                 unsigned char* __restrict__ ws, unsigned short* __restrict__ out) {
  __shared__ __attribute__((aligned(16))) float lds[LDS_BYTES / 4];
  const int tid = threadIdx.x, lane = tid & 63;
  const int wave = __builtin_amdgcn_readfirstlane(tid >> 6);
  const int bid = blockIdx.x;
  const int pair = bid / NSPLIT, split = bid % NSPLIT;
  const bool is_cons = (split == CONS);
  const int slot = split < CONS ? split : split - 1;
  lu32* lw = (lu32*)(lds + L_W);

  if (wave == WAVES) {
    gu64* gpair = (gu64*)(ws + WS_GRAN) + (size_t)pair * 3 * GREC;
    __syncthreads();
    gu32* ep = (gu32*)(ws + (size_t)pair * 128);
    const unsigned epoch = __hip_atomic_load(ep, __ATOMIC_RELAXED, __HIP_MEMORY_SCOPE_AGENT);
    if (!is_cons) {
      gu64* r = gpair + slot * GREC;
#pragma unroll
      for (int i = 0; i < 8; ++i) st_gran(r + i * 64 + lane, 0u, 0.f);
      if (lane < GREC - 512) st_gran(r + 512 + lane, 0u, 0.f);
    }
    unsigned tag = (epoch * 0x9E3779B1u + 0x7F4A7C15u) | 1u;
    if (tag == epoch) tag += 2u;
    if (lane == 0) st_lds(lw + W_TAG, tag);
    asm volatile("s_waitcnt vmcnt(0)" ::: "memory");
    while (__ballot(ld_lds(lw + W_FLAG + (lane & 3)) != 0u) != ~0ull) __builtin_amdgcn_s_sleep(64);
    lds_acquire();
    const float mw = __uint_as_float(ld_lds(lw + W_M + (lane & 7)));
    const float lv = __uint_as_float(ld_lds(lw + W_L + (lane & 7)));
    float Mb = fmaxf(mw, dpp<XOR2>(mw));
    Mb = fmaxf(Mb, __shfl_xor(Mb, 4));
    const float fw = __builtin_amdgcn_exp2f(mw - Mb);
    float Lb = lv * fw;
    Lb += dpp<XOR2>(Lb);
    Lb += __shfl_xor(Lb, 4);
    if (!is_cons) {
      if (lane < 8) st_lds(lw + W_FW + lane, __float_as_uint(fw));
      const float Mh = __shfl(Mb, (lane >> 1) & 1), Lh = __shfl(Lb, (lane >> 1) & 1);
      if (lane < 16) st_gran(gpair + slot * GREC + 512 + lane, tag, (lane & 1) ? Lh : Mh);
      lds_release();
      if (lane == 0) st_lds(lw + W_RDY, 1u);
    } else {
      const int sl = lane < 12 ? lane : 0;
      u64 x = 0;
      for (int spin = 0; spin < SPIN_CAP; ++spin) {
        x = ld_gran(gpair + (sl >> 2) * GREC + 512 + (sl & 3));
        if (__all(gok(x, tag))) break;
        __builtin_amdgcn_s_sleep(32);
      }
      if (lane == 0) __hip_atomic_store(ep, tag, __ATOMIC_RELAXED, __HIP_MEMORY_SCOPE_AGENT);
      const float sv = gval(x);
      const int h = lane & 1;
      float Mr[3], Lr[3];
#pragma unroll
      for (int pr = 0; pr < 3; ++pr) { Mr[pr] = __shfl(sv, pr * 4 + h * 2); Lr[pr] = __shfl(sv, pr * 4 + h * 2 + 1); }
      const float Mt = fmaxf(fmaxf(Mb, Mr[0]), fmaxf(Mr[1], Mr[2]));
      const float fo = __builtin_amdgcn_exp2f(Mb - Mt);
      float Lt = Lb * fo, fr[3];
#pragma unroll
      for (int pr = 0; pr < 3; ++pr) { fr[pr] = __builtin_amdgcn_exp2f(Mr[pr] - Mt); Lt = fmaf(fr[pr], Lr[pr], Lt); }
      const float inv = 1.f / Lt;
      if (lane < 8) st_lds(lw + W_FW + lane, __float_as_uint(fw * fo * inv));
      if (lane < 6)
        st_lds(lw + W_FR + lane, __float_as_uint(((lane >> 1) == 0 ? fr[0] : (lane >> 1) == 1 ? fr[1] : fr[2]) * inv));
      lds_release();
      if (lane == 0) st_lds(lw + W_RDY, 1u);
      while (ld_lds(lw + W_PROG) == 0u) __builtin_amdgcn_s_sleep(32);
      unsigned missing = 7u;
      while (missing != 0u && ld_lds(lw + W_PROG) < 2u) {
        const int rr = lane < 3 ? lane : 0;
        const u64 y = ld_gran(gpair + rr * GREC + 511);
        const unsigned seen = (unsigned)__ballot(gok(y, tag)) & missing;
        if (seen == 0u) { __builtin_amdgcn_s_sleep(16); continue; }
#pragma unroll
        for (int pr = 0; pr < 3; ++pr) {
          if (!((seen >> pr) & 1u)) continue;
          u64 z[8];
#pragma unroll
          for (int i = 0; i < 8; ++i) z[i] = ld_gran(gpair + pr * GREC + i * 64 + lane);
          bool ok = true;
#pragma unroll
          for (int i = 0; i < 8; ++i) ok &= gok(z[i], tag);
          if (__all(ok)) {
#pragma unroll
            for (int hh = 0; hh < 2; ++hh) {
              float* rec = lds + ((WAVES + pr) * 2 + hh) * DH;
              *reinterpret_cast<f32x2*>(rec + 2 * lane) = f32x2{gval(z[hh * 4 + 0]), gval(z[hh * 4 + 2])};
              *reinterpret_cast<f32x2*>(rec + 128 + 2 * lane) = f32x2{gval(z[hh * 4 + 1]), gval(z[hh * 4 + 3])};
            }
            missing &= ~(1u << pr);
            lds_release();
            if (lane == 0) st_lds(lw + W_MISS, missing);
          }
        }
      }
    }
    return;
  }

  const size_t blockrow = (size_t)bid * TPB;
  const size_t waverow = blockrow + wave * U;
  const int nbytes = (TPB - wave * U) * 1024;
  const rsrc_t kp = __builtin_amdgcn_make_buffer_rsrc((void*)(qk + waverow * DH), 0, nbytes, 0x00020000);
  const rsrc_t vp = __builtin_amdgcn_make_buffer_rsrc((void*)(qv + waverow * DH), 0, nbytes, 0x00020000);
  const int voff = lane * 16;

  const size_t gbase = ((size_t)pair * NG + split * NCH) * DH;
  const u32x4 ks_st = __builtin_nontemporal_load(reinterpret_cast<const u32x4*>(ksc + gbase + tid * 8));
  const u32x4 kz_st = __builtin_nontemporal_load(reinterpret_cast<const u32x4*>(kzr + gbase + tid * 8));
  const u32x2 q0 = __builtin_nontemporal_load(reinterpret_cast<const u32x2*>(q + ((size_t)pair * 2 + 0) * DH + lane * 4));
  const u32x2 q1 = __builtin_nontemporal_load(reinterpret_cast<const u32x2*>(q + ((size_t)pair * 2 + 1) * DH + lane * 4));
  const u32x2 vs_st = __builtin_nontemporal_load(reinterpret_cast<const u32x2*>(vsc + blockrow + tid * 4));
  const u32x2 vz_st = __builtin_nontemporal_load(reinterpret_cast<const u32x2*>(vzr + blockrow + tid * 4));

  i32x4 buf[W];
#pragma unroll
  for (int s = 0; s < W; ++s) {
    __builtin_amdgcn_sched_barrier(0);
    buf[s] = ldrow(kp, voff, 0, s);
  }
  __builtin_amdgcn_sched_barrier(0);
  if (tid < 16) st_lds(lw + tid, tid == W_MISS ? 7u : 0u);
  *reinterpret_cast<u32x4*>(lds + L_KS + tid * 4) = ks_st;
  *reinterpret_cast<u32x4*>(lds + L_KZ + tid * 4) = kz_st;
  *reinterpret_cast<u32x2*>(lds + L_VS + tid * 2) = vs_st;
  *reinterpret_cast<u32x2*>(lds + L_VZ + tid * 2) = vz_st;
  __syncthreads();

  const bool b8 = lane & 8, b4 = lane & 4, b2 = lane & 2, b1 = lane & 1;
  float sr[NCH];
#pragma unroll
  for (int i = 0; i < NCH; ++i) sr[i] = 0.f;
  u32x2 ksn = *reinterpret_cast<const u32x2*>(lds + L_KS + lane * 2);
  u32x2 kzn = *reinterpret_cast<const u32x2*>(lds + L_KZ + lane * 2);
  unsigned due = 0;
  int lag = 0;

#pragma unroll 1
  for (int c = 0; c < NCH; ++c) {
    const int sb = c * CT * 1024;
    const bool lastc = (c + 1 == NCH);
    const rsrc_t np = lastc ? vp : kp;
    const int sbn = lastc ? 0 : sb + CT * 1024;
    const f32x2 ksl = unpk(ksn[0]), ksh = unpk(ksn[1]), kzl = unpk(kzn[0]), kzh = unpk(kzn[1]);
    {
      const int cn = lastc ? c : c + 1;
      ksn = *reinterpret_cast<const u32x2*>(lds + L_KS + cn * 128 + lane * 2);
      kzn = *reinterpret_cast<const u32x2*>(lds + L_KZ + cn * 128 + lane * 2);
    }
    const unsigned now = (unsigned)__builtin_amdgcn_s_memrealtime();
    if (c <= 1) due = now;
    { const int nl = (int)(now - due); lag = (nl > lag + JUMP) ? lag : nl; }
    due = now - lag + TAU;
    float lvl[5];
    float sc = 0.f;
#pragma unroll
    for (int s = 0; s < CH; ++s) {
      __builtin_amdgcn_sched_barrier(0);
      const i32x4 x = buf[s % W];
      f32x2 xl = f32x2{(float)x[0], (float)x[1]};
      f32x2 xh = f32x2{(float)x[2], (float)x[3]};
      asm volatile("" : "+v"(xl), "+v"(xh));
      gate(s, lag);
      if (s + W < CH) buf[s % W] = ldrow(kp, voff, sb, s + W);
      else buf[s % W] = ldrow(np, voff, sbn, s + W - CH);
      const unsigned t01 = cvt_pk(xl - kzl), t23 = cvt_pk(xh - kzh);
      const unsigned k01 = cvt_pk(unpk(t01) * ksl), k23 = cvt_pk(unpk(t23) * ksh);
      const float d0 = dot2(k23, q0[1], dot2(k01, q0[0], 0.f));
      const float d1 = dot2(k23, q1[1], dot2(k01, q1[0], 0.f));
      float v = comb32(d0, d1);
      if ((s & 1) == 0) { lvl[0] = v; continue; }
      v = comb16(lvl[0], v);
      if ((s & 2) == 0) { lvl[1] = v; continue; }
      v = combdpp<XOR8>(lvl[1], v, b8);
      if ((s & 4) == 0) { lvl[2] = v; continue; }
      v = combdpp<XOR7>(lvl[2], v, b4);
      if ((s & 8) == 0) { lvl[3] = v; continue; }
      v = combdpp<XOR2>(lvl[3], v, b2);
      if ((s & 16) == 0) { lvl[4] = v; continue; }
      sc = combdpp<XOR1>(lvl[4], v, b1);
    }
    __builtin_amdgcn_sched_barrier(0);
#pragma unroll
    for (int i = 0; i + 1 < NCH; ++i) sr[i] = sr[i + 1];
    sr[NCH - 1] = sc;
  }
  const int jl = brev5(lane & 31);
  const int tl = (jl / U) * U * WAVES + (jl % U) + wave * U;
  const unsigned short* lvs = reinterpret_cast<const unsigned short*>(lds + L_VS) + tl;
  const unsigned short* lvz = reinterpret_cast<const unsigned short*>(lds + L_VZ) + tl;
  constexpr float C2 = 0.0625f * 1.4426950408889634f;
  float m = -1e38f;
#pragma unroll
  for (int i = 0; i < NCH; i += 2) {
    const unsigned r = cvt_pk(f32x2{sr[i], sr[i + 1]});
    sr[i] = bf_lo(r) * C2; sr[i + 1] = bf_hi(r) * C2;
    m = fmaxf(m, fmaxf(sr[i], sr[i + 1]));
  }
  m = half_max(m);
  float l = 0.f, cc = 0.f;
#pragma unroll
  for (int i = 0; i < NCH; ++i) {
    const float p = __builtin_amdgcn_exp2f(sr[i] - m);
    l += p;
    sr[i] = p * bf_lo(lvs[i * CT]);
    cc = fmaf(sr[i], bf_lo(lvz[i * CT]), cc);
  }
  const float L = half_sum(l), C = half_sum(cc);
  const float c0 = rdlane(C, 0), c1 = rdlane(C, 32);
  if ((lane & 31) == 0) {
    st_lds(lw + W_M + wave * 2 + (lane >> 5), __float_as_uint(m));
    st_lds(lw + W_L + wave * 2 + (lane >> 5), __float_as_uint(L));
  }
  lds_release();
  if (lane == 0) st_lds(lw + W_FLAG + wave, 1u);
  f32x2 a0l = {-c0, -c0}, a0h = a0l, a1l = {-c1, -c1}, a1h = a1l;
  due += 30;
  float f0 = 0.f, f1 = 0.f;
  unsigned tag = 0u;
#pragma unroll 1
  for (int c = 0; c < NCH; ++c) {
    const int sb = c * CT * 1024;
    const int sbn = sb + CT * 1024;
    const float w = sr[0];
    if (c == POLLC && tid == 0) st_lds(lw + W_PROG, 1u);
    if (c == NCH - 1) {
      while (ld_lds(lw + W_RDY) == 0u) __builtin_amdgcn_s_sleep(2);
      lds_acquire();
      f0 = __uint_as_float(ld_lds(lw + W_FW + wave * 2)); f1 = __uint_as_float(ld_lds(lw + W_FW + wave * 2 + 1));
      tag = ld_lds(lw + W_TAG);
    }
    const unsigned now = (unsigned)__builtin_amdgcn_s_memrealtime();
    { const int nl = (int)(now - due); lag = (nl > lag + JUMP) ? lag : nl; }
    due = now - lag + TAU;
#pragma unroll
    for (int j = 0; j < CH; ++j) {
      __builtin_amdgcn_sched_barrier(0);
      const i32x4 x = buf[j % W];
      f32x2 xl = f32x2{(float)x[0], (float)x[1]};
      f32x2 xh = f32x2{(float)x[2], (float)x[3]};
      asm volatile("" : "+v"(xl), "+v"(xh));
      gate(j, lag);
      if (j + W < CH) buf[j % W] = ldrow(vp, voff, sb, j + W);
      else buf[j % W] = ldrow(vp, voff, sbn, j + W - CH);
      const float w0 = rdlane(w, brev5(j)), w1 = rdlane(w, 32 + brev5(j));
      const f32x2 w0v = {w0, w0}, w1v = {w1, w1};
      a0l = __builtin_elementwise_fma(w0v, xl, a0l);
      a0h = __builtin_elementwise_fma(w0v, xh, a0h);
      a1l = __builtin_elementwise_fma(w1v, xl, a1l);
      a1h = __builtin_elementwise_fma(w1v, xh, a1h);
      asm volatile("" : "+v"(a0l), "+v"(a0h), "+v"(a1l), "+v"(a1h));
    }
#pragma unroll
    for (int i = 0; i + 1 < NCH; ++i) sr[i] = sr[i + 1];
  }
  if (tid == 0) st_lds(lw + W_PROG, 2u);
  *reinterpret_cast<f32x4*>(lds + (wave * 2 + 0) * DH + lane * 4) = f32x4{a0l[0], a0l[1], a0h[0], a0h[1]} * f0;
  *reinterpret_cast<f32x4*>(lds + (wave * 2 + 1) * DH + lane * 4) = f32x4{a1l[0], a1l[1], a1h[0], a1h[1]} * f1;
  lds_release();
  if (lane == 0) __hip_atomic_fetch_add(lw + W_CNT, 1u, __ATOMIC_RELAXED, __HIP_MEMORY_SCOPE_WORKGROUP);
  while (ld_lds(lw + W_CNT) < (unsigned)WAVES) {}
  const unsigned miss0 = ld_lds(lw + W_MISS);
  lds_acquire();
  const int oh = tid >> 7, oi = tid & 127, od = oi * 2;
  f32x2 o = {0.f, 0.f};
#pragma unroll
  for (int w = 0; w < WAVES; ++w) o += *reinterpret_cast<const f32x2*>(lds + (w * 2 + oh) * DH + od);
  gu64* g0 = (gu64*)(ws + WS_GRAN) + (size_t)pair * 3 * GREC + oh * 256 + oi;
  if (!is_cons) {
    st_gran(g0 + slot * GREC, tag, o[0]);
    st_gran(g0 + slot * GREC + 128, tag, o[1]);
  } else {
    unsigned missing = __builtin_amdgcn_readfirstlane(miss0);
    f32x2 ro[3];
#pragma unroll
    for (int pr = 0; pr < 3; ++pr) ro[pr] = *reinterpret_cast<const f32x2*>(lds + ((WAVES + pr) * 2 + oh) * DH + od);
    for (int spin = 0; missing != 0u && spin < SPIN_CAP; ++spin) {
      u64 x[3][2];
#pragma unroll
      for (int pr = 0; pr < 3; ++pr) {
        x[pr][0] = x[pr][1] = 0;
        if ((missing >> pr) & 1u) { x[pr][0] = ld_gran(g0 + pr * GREC); x[pr][1] = ld_gran(g0 + pr * GREC + 128); }
      }
#pragma unroll
      for (int pr = 0; pr < 3; ++pr) {
        if (__all(gok(x[pr][0], tag) && gok(x[pr][1], tag))) {
          ro[pr] = f32x2{gval(x[pr][0]), gval(x[pr][1])};
          missing &= ~(1u << pr);
        }
      }
      if (missing != 0u) __builtin_amdgcn_s_sleep(1);
    }
#pragma unroll
    for (int pr = 0; pr < 3; ++pr) o += ro[pr] * __uint_as_float(ld_lds(lw + W_FR + pr * 2 + oh));
    *reinterpret_cast<unsigned*>(out + ((size_t)pair * 2 + oh) * DH + od) = cvt_pk(o);
  }
}

}

extern "C" void kernel_launch(void* const* d_in, const int* in_sizes, int n_in, void* d_out, int out_size, void* d_ws,
                              size_t ws_size, hipStream_t stream) {
  const unsigned short* q = (const unsigned short*)d_in[0];
  const int* qk = (const int*)d_in[1];
  const int* qv = (const int*)d_in[2];
  const unsigned short* ks = (const unsigned short*)d_in[3];
  const unsigned short* kz = (const unsigned short*)d_in[4];
  const unsigned short* vs = (const unsigned short*)d_in[5];
  const unsigned short* vz = (const unsigned short*)d_in[6];
  kivi_decode_attn<<<dim3(NPAIR * NSPLIT), dim3((WAVES + 1) * 64), 0, stream>>>(
      q, qk, qv, ks, kz, vs, vz, (unsigned char*)d_ws, (unsigned short*)d_out);
}
```

```cpp
#include <hip/hip_runtime.h>
#include <hip/hip_bf16.h>
#include <stdint.h>

namespace {

constexpr int B = 8, HQ = 16, HKV = 8, SKV = 4096, DH = 256, G = 128, NG = SKV / G, REP = HQ / HKV;
constexpr int WAVES = 4;
constexpr int CH = 32;
constexpr int W = 8;
constexpr int U = 4;
constexpr int TPB = 1024;
constexpr int CT = CH * WAVES;
constexpr int NCH = TPB / CT;
constexpr int NSPLIT = SKV / TPB;
constexpr int NPAIR = B * HKV;
constexpr int D = 5;
constexpr int BOOST = 2;
constexpr int TAU = 438;
constexpr int JUMP = 120;
constexpr int CONS = 0;
constexpr int POLLC = 6;
constexpr int SPIN_CAP = 1 << 20;
constexpr int LDS_BYTES = 96 * 1024;
static_assert(CT == G, "a chunk is one K group");
static_assert(D < W && CH % W == 0 && W % U == 0, "ring");
static_assert(REP == 2 && NSPLIT == 4 && NCH == 8, "two query heads per kv head, four splits");

typedef int i32x4 __attribute__((ext_vector_type(4)));
typedef unsigned u32x2 __attribute__((ext_vector_type(2)));
typedef unsigned u32x4 __attribute__((ext_vector_type(4)));
typedef float f32x2 __attribute__((ext_vector_type(2)));
typedef float f32x4 __attribute__((ext_vector_type(4)));
typedef __bf16 bf16x2 __attribute__((ext_vector_type(2)));
typedef __amdgpu_buffer_rsrc_t rsrc_t;
typedef unsigned long long u64;
typedef __attribute__((address_space(1))) u64 gu64;
typedef __attribute__((address_space(1))) unsigned gu32;
typedef __attribute__((address_space(3))) unsigned lu32;

__device__ __forceinline__ float bf_lo(unsigned u) { return __builtin_bit_cast(float, u << 16); }
__device__ __forceinline__ float bf_hi(unsigned u) { return __builtin_bit_cast(float, u & 0xffff0000u); }
__device__ __forceinline__ unsigned cvt_pk(f32x2 v) {
  bf16x2 r = __builtin_convertvector(v, bf16x2);
  return __builtin_bit_cast(unsigned, r);
}
__device__ __forceinline__ f32x2 unpk(unsigned u) { return f32x2{bf_lo(u), bf_hi(u)}; }
__device__ __forceinline__ float dot2(unsigned a, unsigned b, float c) {
  return __builtin_amdgcn_fdot2_f32_bf16(__builtin_bit_cast(bf16x2, a), __builtin_bit_cast(bf16x2, b), c, false);
}
template <int CTRL> __device__ __forceinline__ float dpp(float x) {
  return __builtin_bit_cast(float, __builtin_amdgcn_mov_dpp(__builtin_bit_cast(int, x), CTRL, 0xf, 0xf, true));
}
constexpr int XOR1 = 0xB1, XOR2 = 0x4E, XOR7 = 0x141, XOR8 = 0x128;

__device__ __forceinline__ float comb32(float a, float b) {
  auto s = __builtin_amdgcn_permlane32_swap(__float_as_uint(a), __float_as_uint(b), false, false);
  return __uint_as_float(s[0]) + __uint_as_float(s[1]);
}
__device__ __forceinline__ float comb16(float a, float b) {
  auto s = __builtin_amdgcn_permlane16_swap(__float_as_uint(a), __float_as_uint(b), false, false);
  return __uint_as_float(s[0]) + __uint_as_float(s[1]);
}
template <int CTRL> __device__ __forceinline__ float combdpp(float a, float b, bool hi) {
  const float keep = hi ? b : a, send = hi ? a : b;
  return keep + dpp<CTRL>(send);
}
__device__ __forceinline__ float half_max(float x) {
  x = fmaxf(x, dpp<XOR1>(x));
  x = fmaxf(x, dpp<XOR2>(x));
  x = fmaxf(x, dpp<XOR7>(x));
  x = fmaxf(x, dpp<XOR8>(x));
  auto s = __builtin_amdgcn_permlane16_swap(__float_as_uint(x), __float_as_uint(x), false, false);
  return fmaxf(__uint_as_float(s[0]), __uint_as_float(s[1]));
}
__device__ __forceinline__ float half_sum(float x) {
  x += dpp<XOR1>(x);
  x += dpp<XOR2>(x);
  x += dpp<XOR7>(x);
  x += dpp<XOR8>(x);
  auto s = __builtin_amdgcn_permlane16_swap(__float_as_uint(x), __float_as_uint(x), false, false);
  return __uint_as_float(s[0]) + __uint_as_float(s[1]);
}
__device__ __forceinline__ float rdlane(float x, int l) {
  return __builtin_bit_cast(float, __builtin_amdgcn_readlane(__builtin_bit_cast(int, x), l));
}
__host__ __device__ constexpr int roff(int j) { return ((j / U) * U * WAVES + (j % U)) * 1024; }
__device__ __forceinline__ i32x4 ldrow(rsrc_t r, int voff, int sbase, int j) {
  return __builtin_amdgcn_raw_buffer_load_b128(r, voff + (roff(j) & 4095), sbase + (roff(j) & ~4095), 2  );
}
__host__ __device__ constexpr int brev5(int j) {
  return ((j & 1) << 4) | ((j & 2) << 2) | (j & 4) | ((j & 8) >> 2) | ((j & 16) >> 4);
}
__device__ __forceinline__ void gate(int s, int lag) {
  const int ph = (s & 3) == 0 ? 0 : (s & 3) == 2 ? 1 : (s & 3) == 1 ? 2 : 3;
  if (ph < BOOST) {
    asm volatile("s_cmp_gt_i32 %0, 0\n\ts_cbranch_scc1 1f\n\ts_waitcnt vmcnt(%1)\n1:\n\ts_waitcnt vmcnt(%2)"
                 :: "s"(lag), "n"(D - 1), "n"(D) : "memory", "scc");
  } else {
    asm volatile("s_waitcnt vmcnt(%0)" :: "n"(D - 1) : "memory");
  }
}
__device__ __forceinline__ void st_gran(gu64* p, unsigned tag, float v) {
  __hip_atomic_store(p, ((u64)tag << 32) | (u64)__float_as_uint(v), __ATOMIC_RELAXED, __HIP_MEMORY_SCOPE_AGENT);
}
__device__ __forceinline__ u64 ld_gran(gu64* p) {
  return __hip_atomic_load(p, __ATOMIC_RELAXED, __HIP_MEMORY_SCOPE_AGENT);
}
__device__ __forceinline__ float gval(u64 x) { return __uint_as_float((unsigned)x); }
__device__ __forceinline__ bool gok(u64 x, unsigned tag) { return (unsigned)(x >> 32) == tag; }
__device__ __forceinline__ unsigned ld_lds(lu32* p) {
  return __hip_atomic_load(p, __ATOMIC_RELAXED, __HIP_MEMORY_SCOPE_WORKGROUP);
}
__device__ __forceinline__ void st_lds(lu32* p, unsigned v) {
  __hip_atomic_store(p, v, __ATOMIC_RELAXED, __HIP_MEMORY_SCOPE_WORKGROUP);
}
__device__ __forceinline__ void lds_release() { __builtin_amdgcn_fence(__ATOMIC_RELEASE, "workgroup", "local"); }
__device__ __forceinline__ void lds_acquire() { __builtin_amdgcn_fence(__ATOMIC_ACQUIRE, "workgroup", "local"); }

constexpr int L_KS = 7 * 2 * DH, L_KZ = L_KS + 1024, L_VS = L_KZ + 1024, L_VZ = L_VS + 512, L_W = L_VZ + 512;
constexpr int W_PROG = 1, W_MISS = 2, W_RDY = 3, W_FLAG = 4, W_CNT = 8, W_TAG = 12, W_M = 16, W_L = 24, W_FW = 32,
              W_FR = 40, L_END = L_W + 48;
static_assert(L_END * 4 <= LDS_BYTES, "LDS");
constexpr int GREC = 528, G_ST = 512;
constexpr size_t WS_GRAN = 16384;

__global__ void __launch_bounds__((WAVES + 1) * 64)
kivi_decode_attn(const unsigned short* __restrict__ q, const int* __restrict__ qk, const int* __restrict__ qv,
                 const unsigned short* __restrict__ ksc, const unsigned short* __restrict__ kzr,
                 const unsigned short* __restrict__ vsc, const unsigned short* __restrict__ vzr,
                 unsigned char* __restrict__ ws, unsigned short* __restrict__ out) {
  __shared__ __attribute__((aligned(16))) float lds[LDS_BYTES / 4];
  const int tid = threadIdx.x, lane = tid & 63;
  const int wave = __builtin_amdgcn_readfirstlane(tid >> 6);
  const int bid = blockIdx.x;
  const int pair = bid / NSPLIT, split = bid % NSPLIT;
  const bool is_cons = (split == CONS);
  const int slot = split < CONS ? split : split - 1;
  lu32* lw = (lu32*)(lds + L_W);

  if (wave == WAVES) {
    gu64* gpair = (gu64*)(ws + WS_GRAN) + (size_t)pair * 3 * GREC;
    __syncthreads();
    gu32* ep = (gu32*)(ws + (size_t)pair * 128);
    const unsigned epoch = __hip_atomic_load(ep, __ATOMIC_RELAXED, __HIP_MEMORY_SCOPE_AGENT);
    const unsigned oldo = (unsigned)(ld_gran(gpair + (is_cons ? 0 : slot) * GREC) >> 32);
    if (!is_cons && lane < 8) st_gran(gpair + slot * GREC + G_ST + lane, 0u, 0.f);
    unsigned tag = (epoch * 0x9E3779B1u + 0x7F4A7C15u) | 1u;
    if (tag == epoch) tag += 2u;
    asm volatile("s_waitcnt vmcnt(0)" ::: "memory");
    while (__ballot(ld_lds(lw + W_FLAG + (lane & 3)) != 0u) != ~0ull) __builtin_amdgcn_s_sleep(64);
    lds_acquire();
    const float mw = __uint_as_float(ld_lds(lw + W_M + (lane & 7)));
    const float lv = __uint_as_float(ld_lds(lw + W_L + (lane & 7)));
    float Mb = fmaxf(mw, dpp<XOR2>(mw));
    Mb = fmaxf(Mb, __shfl_xor(Mb, 4));
    const float fw = __builtin_amdgcn_exp2f(mw - Mb);
    float Lb = lv * fw;
    Lb += dpp<XOR2>(Lb);
    Lb += __shfl_xor(Lb, 4);
    if (!is_cons) {
      if (lane < 8) st_lds(lw + W_FW + lane, __float_as_uint(fw));
      unsigned otag = ((oldo ^ tag) * 0x85EBCA6Bu + 0x2545F491u) | 1u;
      if (otag == oldo) otag += 2u;
      if (lane == 0) st_lds(lw + W_TAG, otag);
      const float Mh = __shfl(Mb, (lane >> 1) & 1), Lh = __shfl(Lb, (lane >> 1) & 1);
      if (lane < 8)
        st_gran(gpair + slot * GREC + G_ST + lane, tag, (lane & 4) ? __uint_as_float(otag) : (lane & 1) ? Lh : Mh);
      lds_release();
      if (lane == 0) st_lds(lw + W_RDY, 1u);
    } else {
      const int sl = lane < 24 ? lane : 0;
      u64 x = 0;
      for (int spin = 0; spin < SPIN_CAP; ++spin) {
        x = ld_gran(gpair + (sl >> 3) * GREC + G_ST + (sl & 7));
        if (__all(gok(x, tag))) break;
        __builtin_amdgcn_s_sleep(32);
      }
      if (lane == 0) __hip_atomic_store(ep, tag, __ATOMIC_RELAXED, __HIP_MEMORY_SCOPE_AGENT);
      if (lane < 24) st_gran(gpair + (sl >> 3) * GREC + G_ST + (sl & 7), 0u, 0.f);
      const float sv = gval(x);
      const int h = lane & 1;
      float Mr[3], Lr[3];
#pragma unroll
      for (int pr = 0; pr < 3; ++pr) { Mr[pr] = __shfl(sv, pr * 8 + h * 2); Lr[pr] = __shfl(sv, pr * 8 + h * 2 + 1); }
      unsigned otag[3];
#pragma unroll
      for (int pr = 0; pr < 3; ++pr) otag[pr] = __builtin_amdgcn_readlane((unsigned)x, pr * 8 + 4);
      const unsigned myot = lane == 1 ? otag[1] : lane == 2 ? otag[2] : otag[0];
      if (lane < 3) st_lds(lw + W_TAG + lane, myot);
      const float Mt = fmaxf(fmaxf(Mb, Mr[0]), fmaxf(Mr[1], Mr[2]));
      const float fo = __builtin_amdgcn_exp2f(Mb - Mt);
      float Lt = Lb * fo, fr[3];
#pragma unroll
      for (int pr = 0; pr < 3; ++pr) { fr[pr] = __builtin_amdgcn_exp2f(Mr[pr] - Mt); Lt = fmaf(fr[pr], Lr[pr], Lt); }
      const float inv = 1.f / Lt;
      if (lane < 8) st_lds(lw + W_FW + lane, __float_as_uint(fw * fo * inv));
      if (lane < 6)
        st_lds(lw + W_FR + lane, __float_as_uint(((lane >> 1) == 0 ? fr[0] : (lane >> 1) == 1 ? fr[1] : fr[2]) * inv));
      lds_release();
      if (lane == 0) st_lds(lw + W_RDY, 1u);
      while (ld_lds(lw + W_PROG) == 0u) __builtin_amdgcn_s_sleep(32);
      unsigned missing = 7u;
      while (missing != 0u && ld_lds(lw + W_PROG) < 2u) {
        const int rr = lane < 3 ? lane : 0;
        const u64 y = ld_gran(gpair + rr * GREC + 511);
        const unsigned seen = (unsigned)__ballot(gok(y, myot)) & missing;
        if (seen == 0u) { __builtin_amdgcn_s_sleep(16); continue; }
#pragma unroll
        for (int pr = 0; pr < 3; ++pr) {
          if (!((seen >> pr) & 1u)) continue;
          u64 z[8];
#pragma unroll
          for (int i = 0; i < 8; ++i) z[i] = ld_gran(gpair + pr * GREC + i * 64 + lane);
          bool ok = true;
#pragma unroll
          for (int i = 0; i < 8; ++i) ok &= gok(z[i], otag[pr]);
          if (__all(ok)) {
#pragma unroll
            for (int hh = 0; hh < 2; ++hh) {
              float* rec = lds + ((WAVES + pr) * 2 + hh) * DH;
              *reinterpret_cast<f32x2*>(rec + 2 * lane) = f32x2{gval(z[hh * 4 + 0]), gval(z[hh * 4 + 2])};
              *reinterpret_cast<f32x2*>(rec + 128 + 2 * lane) = f32x2{gval(z[hh * 4 + 1]), gval(z[hh * 4 + 3])};
            }
            missing &= ~(1u << pr);
            lds_release();
            if (lane == 0) st_lds(lw + W_MISS, missing);
          }
        }
      }
    }
    return;
  }

  const size_t blockrow = (size_t)bid * TPB;
  const size_t waverow = blockrow + wave * U;
  const int nbytes = (TPB - wave * U) * 1024;
  const rsrc_t kp = __builtin_amdgcn_make_buffer_rsrc((void*)(qk + waverow * DH), 0, nbytes, 0x00020000);
  const rsrc_t vp = __builtin_amdgcn_make_buffer_rsrc((void*)(qv + waverow * DH), 0, nbytes, 0x00020000);
  const int voff = lane * 16;

  const size_t gbase = ((size_t)pair * NG + split * NCH) * DH;
  const u32x4 ks_st = __builtin_nontemporal_load(reinterpret_cast<const u32x4*>(ksc + gbase + tid * 8));
  const u32x4 kz_st = __builtin_nontemporal_load(reinterpret_cast<const u32x4*>(kzr + gbase + tid * 8));
  const u32x2 q0 = __builtin_nontemporal_load(reinterpret_cast<const u32x2*>(q + ((size_t)pair * 2 + 0) * DH + lane * 4));
  const u32x2 q1 = __builtin_nontemporal_load(reinterpret_cast<const u32x2*>(q + ((size_t)pair * 2 + 1) * DH + lane * 4));
  const u32x2 vs_st = __builtin_nontemporal_load(reinterpret_cast<const u32x2*>(vsc + blockrow + tid * 4));
  const u32x2 vz_st = __builtin_nontemporal_load(reinterpret_cast<const u32x2*>(vzr + blockrow + tid * 4));

  i32x4 buf[W];
#pragma unroll
  for (int s = 0; s < W; ++s) {
    __builtin_amdgcn_sched_barrier(0);
    buf[s] = ldrow(kp, voff, 0, s);
  }
  __builtin_amdgcn_sched_barrier(0);
  if (tid < 16) st_lds(lw + tid, tid == W_MISS ? 7u : 0u);
  *reinterpret_cast<u32x4*>(lds + L_KS + tid * 4) = ks_st;
  *reinterpret_cast<u32x4*>(lds + L_KZ + tid * 4) = kz_st;
  *reinterpret_cast<u32x2*>(lds + L_VS + tid * 2) = vs_st;
  *reinterpret_cast<u32x2*>(lds + L_VZ + tid * 2) = vz_st;
  __syncthreads();

  const bool b8 = lane & 8, b4 = lane & 4, b2 = lane & 2, b1 = lane & 1;
  float sr[NCH];
#pragma unroll
  for (int i = 0; i < NCH; ++i) sr[i] = 0.f;
  u32x2 ksn = *reinterpret_cast<const u32x2*>(lds + L_KS + lane * 2);
  u32x2 kzn = *reinterpret_cast<const u32x2*>(lds + L_KZ + lane * 2);
  unsigned due = 0;
  int lag = 0;

#pragma unroll 1
  for (int c = 0; c < NCH; ++c) {
    const int sb = c * CT * 1024;
    const bool lastc = (c + 1 == NCH);
    const rsrc_t np = lastc ? vp : kp;
    const int sbn = lastc ? 0 : sb + CT * 1024;
    const f32x2 ksl = unpk(ksn[0]), ksh = unpk(ksn[1]), kzl = unpk(kzn[0]), kzh = unpk(kzn[1]);
    {
      const int cn = lastc ? c : c + 1;
      ksn = *reinterpret_cast<const u32x2*>(lds + L_KS + cn * 128 + lane * 2);
      kzn = *reinterpret_cast<const u32x2*>(lds + L_KZ + cn * 128 + lane * 2);
    }
    const unsigned now = (unsigned)__builtin_amdgcn_s_memrealtime();
    if (c <= 1) due = now;
    { const int nl = (int)(now - due); lag = (nl > lag + JUMP) ? lag : nl; }
    due = now - lag + TAU;
    float lvl[5];
    float sc = 0.f;
#pragma unroll
    for (int s = 0; s < CH; ++s) {
      __builtin_amdgcn_sched_barrier(0);
      const i32x4 x = buf[s % W];
      f32x2 xl = f32x2{(float)x[0], (float)x[1]};
      f32x2 xh = f32x2{(float)x[2], (float)x[3]};
      asm volatile("" : "+v"(xl), "+v"(xh));
      gate(s, lag);
      if (s + W < CH) buf[s % W] = ldrow(kp, voff, sb, s + W);
      else buf[s % W] = ldrow(np, voff, sbn, s + W - CH);
      const unsigned t01 = cvt_pk(xl - kzl), t23 = cvt_pk(xh - kzh);
      const unsigned k01 = cvt_pk(unpk(t01) * ksl), k23 = cvt_pk(unpk(t23) * ksh);
      const float d0 = dot2(k23, q0[1], dot2(k01, q0[0], 0.f));
      const float d1 = dot2(k23, q1[1], dot2(k01, q1[0], 0.f));
      float v = comb32(d0, d1);
      if ((s & 1) == 0) { lvl[0] = v; continue; }
      v = comb16(lvl[0], v);
      if ((s & 2) == 0) { lvl[1] = v; continue; }
      v = combdpp<XOR8>(lvl[1], v, b8);
      if ((s & 4) == 0) { lvl[2] = v; continue; }
      v = combdpp<XOR7>(lvl[2], v, b4);
      if ((s & 8) == 0) { lvl[3] = v; continue; }
      v = combdpp<XOR2>(lvl[3], v, b2);
      if ((s & 16) == 0) { lvl[4] = v; continue; }
      sc = combdpp<XOR1>(lvl[4], v, b1);
    }
    __builtin_amdgcn_sched_barrier(0);
#pragma unroll
    for (int i = 0; i + 1 < NCH; ++i) sr[i] = sr[i + 1];
    sr[NCH - 1] = sc;
  }
  const int jl = brev5(lane & 31);
  const int tl = (jl / U) * U * WAVES + (jl % U) + wave * U;
  const unsigned short* lvs = reinterpret_cast<const unsigned short*>(lds + L_VS) + tl;
  const unsigned short* lvz = reinterpret_cast<const unsigned short*>(lds + L_VZ) + tl;
  constexpr float C2 = 0.0625f * 1.4426950408889634f;
  float m = -1e38f;
#pragma unroll
  for (int i = 0; i < NCH; i += 2) {
    const unsigned r = cvt_pk(f32x2{sr[i], sr[i + 1]});
    sr[i] = bf_lo(r) * C2; sr[i + 1] = bf_hi(r) * C2;
    m = fmaxf(m, fmaxf(sr[i], sr[i + 1]));
  }
  m = half_max(m);
  float l = 0.f, cc = 0.f;
#pragma unroll
  for (int i = 0; i < NCH; ++i) {
    const float p = __builtin_amdgcn_exp2f(sr[i] - m);
    l += p;
    sr[i] = p * bf_lo(lvs[i * CT]);
    cc = fmaf(sr[i], bf_lo(lvz[i * CT]), cc);
  }
  const float L = half_sum(l), C = half_sum(cc);
  const float c0 = rdlane(C, 0), c1 = rdlane(C, 32);
  if ((lane & 31) == 0) {
    st_lds(lw + W_M + wave * 2 + (lane >> 5), __float_as_uint(m));
    st_lds(lw + W_L + wave * 2 + (lane >> 5), __float_as_uint(L));
  }
  lds_release();
  if (lane == 0) st_lds(lw + W_FLAG + wave, 1u);
  f32x2 a0l = {-c0, -c0}, a0h = a0l, a1l = {-c1, -c1}, a1h = a1l;
  due += 30;
  float f0 = 0.f, f1 = 0.f;
  unsigned otag[3] = {0u, 0u, 0u};
#pragma unroll 1
  for (int c = 0; c < NCH; ++c) {
    const int sb = c * CT * 1024;
    const int sbn = sb + CT * 1024;
    const float w = sr[0];
    if (c == POLLC && tid == 0) st_lds(lw + W_PROG, 1u);
    if (c == NCH - 1) {
      while (ld_lds(lw + W_RDY) == 0u) __builtin_amdgcn_s_sleep(2);
      lds_acquire();
      f0 = __uint_as_float(ld_lds(lw + W_FW + wave * 2)); f1 = __uint_as_float(ld_lds(lw + W_FW + wave * 2 + 1));
#pragma unroll
      for (int pr = 0; pr < 3; ++pr) otag[pr] = ld_lds(lw + W_TAG + pr);
    }
    const unsigned now = (unsigned)__builtin_amdgcn_s_memrealtime();
    { const int nl = (int)(now - due); lag = (nl > lag + JUMP) ? lag : nl; }
    due = now - lag + TAU;
#pragma unroll
    for (int j = 0; j < CH; ++j) {
      __builtin_amdgcn_sched_barrier(0);
      const i32x4 x = buf[j % W];
      f32x2 xl = f32x2{(float)x[0], (float)x[1]};
      f32x2 xh = f32x2{(float)x[2], (float)x[3]};
      asm volatile("" : "+v"(xl), "+v"(xh));
      gate(j, lag);
      if (j + W < CH) buf[j % W] = ldrow(vp, voff, sb, j + W);
      else buf[j % W] = ldrow(vp, voff, sbn, j + W - CH);
      const float w0 = rdlane(w, brev5(j)), w1 = rdlane(w, 32 + brev5(j));
      const f32x2 w0v = {w0, w0}, w1v = {w1, w1};
      a0l = __builtin_elementwise_fma(w0v, xl, a0l);
      a0h = __builtin_elementwise_fma(w0v, xh, a0h);
      a1l = __builtin_elementwise_fma(w1v, xl, a1l);
      a1h = __builtin_elementwise_fma(w1v, xh, a1h);
      asm volatile("" : "+v"(a0l), "+v"(a0h), "+v"(a1l), "+v"(a1h));
    }
#pragma unroll
    for (int i = 0; i + 1 < NCH; ++i) sr[i] = sr[i + 1];
  }
  if (tid == 0) st_lds(lw + W_PROG, 2u);
  *reinterpret_cast<f32x4*>(lds + (wave * 2 + 0) * DH + lane * 4) = f32x4{a0l[0], a0l[1], a0h[0], a0h[1]} * f0;
  *reinterpret_cast<f32x4*>(lds + (wave * 2 + 1) * DH + lane * 4) = f32x4{a1l[0], a1l[1], a1h[0], a1h[1]} * f1;
  lds_release();
  if (lane == 0) __hip_atomic_fetch_add(lw + W_CNT, 1u, __ATOMIC_RELAXED, __HIP_MEMORY_SCOPE_WORKGROUP);
  while (ld_lds(lw + W_CNT) < (unsigned)WAVES) {}
  const unsigned miss0 = ld_lds(lw + W_MISS);
  lds_acquire();
  const int oh = tid >> 7, oi = tid & 127, od = oi * 2;
  f32x2 o = {0.f, 0.f};
#pragma unroll
  for (int w = 0; w < WAVES; ++w) o += *reinterpret_cast<const f32x2*>(lds + (w * 2 + oh) * DH + od);
  gu64* g0 = (gu64*)(ws + WS_GRAN) + (size_t)pair * 3 * GREC + oh * 256 + oi;
  if (!is_cons) {
    st_gran(g0 + slot * GREC, otag[0], o[0]);
    st_gran(g0 + slot * GREC + 128, otag[0], o[1]);
  } else {
    unsigned missing = __builtin_amdgcn_readfirstlane(miss0);
    f32x2 ro[3];
#pragma unroll
    for (int pr = 0; pr < 3; ++pr) ro[pr] = *reinterpret_cast<const f32x2*>(lds + ((WAVES + pr) * 2 + oh) * DH + od);
    for (int spin = 0; missing != 0u && spin < SPIN_CAP; ++spin) {
      u64 x[3][2];
#pragma unroll
      for (int pr = 0; pr < 3; ++pr) {
        x[pr][0] = x[pr][1] = 0;
        if ((missing >> pr) & 1u) { x[pr][0] = ld_gran(g0 + pr * GREC); x[pr][1] = ld_gran(g0 + pr * GREC + 128); }
      }
#pragma unroll
      for (int pr = 0; pr < 3; ++pr) {
        if (__all(gok(x[pr][0], otag[pr]) && gok(x[pr][1], otag[pr]))) {
          ro[pr] = f32x2{gval(x[pr][0]), gval(x[pr][1])};
          missing &= ~(1u << pr);
        }
      }
      if (missing != 0u) __builtin_amdgcn_s_sleep(1);
    }
#pragma unroll
    for (int pr = 0; pr < 3; ++pr) o += ro[pr] * __uint_as_float(ld_lds(lw + W_FR + pr * 2 + oh));
    *reinterpret_cast<unsigned*>(out + ((size_t)pair * 2 + oh) * DH + od) = cvt_pk(o);
  }
}

}

extern "C" void kernel_launch(void* const* d_in, const int* in_sizes, int n_in, void* d_out, int out_size, void* d_ws,
                              size_t ws_size, hipStream_t stream) {
  const unsigned short* q = (const unsigned short*)d_in[0];
  const int* qk = (const int*)d_in[1];
  const int* qv = (const int*)d_in[2];
  const unsigned short* ks = (const unsigned short*)d_in[3];
  const unsigned short* kz = (const unsigned short*)d_in[4];
  const unsigned short* vs = (const unsigned short*)d_in[5];
  const unsigned short* vz = (const unsigned short*)d_in[6];
  kivi_decode_attn<<<dim3(NPAIR * NSPLIT), dim3((WAVES + 1) * 64), 0, stream>>>(
      q, qk, qv, ks, kz, vs, vz, (unsigned char*)d_ws, (unsigned short*)d_out);
}
```
